```python
import torch

def _softmax(x, axis=-1):
    return torch.softmax(x, dim=axis)
def _gelu(x):
    return torch.nn.functional.gelu(x, approximate="tanh")
def _silu(x):
    return torch.nn.functional.silu(x)
def _sigmoid(x):
    return torch.sigmoid(x)
def _layernorm(x, axis=-1, eps=1e-5):
    m = x.mean(dim=axis, keepdim=True)
    v = x.var(dim=axis, keepdim=True, unbiased=False)
    return (x - m) / torch.sqrt(v + eps)
def _scatter_add(tgt, idx, src, axis=0):
    return tgt.clone().index_add_(axis, idx, src)
def _segment_sum(src, seg, n):
    out = torch.zeros((n,) + src.shape[1:], dtype=src.dtype)
    return out.index_add_(0, seg, src)
def _pad(x, pw):
    flat = tuple(v for pair in reversed(pw) for v in pair)
    return torch.nn.functional.pad(x, flat)

kernel_name = 'gated_mix__sliding_block_cached__swiglu_ffn__decode'

def setup_inputs():

    _w = (lambda a: (lambda t: t.to(torch.bfloat16) if t.is_floating_point() else t)(torch.as_tensor(a)))
    def _A_setup():
        B, Sq, D = 8, 1, 576
        Hq, Hkv, Sk, Dh = 32, 16, 4096, 128
        assert Hq % Hkv == 0, (Hq, Hkv)
        return {
            "x": _w((lambda *s: torch.randn(*s, dtype=torch.bfloat16) * 0.05)(B, Sq, D)),
            "k_cache": _w((lambda *s: torch.randn(*s, dtype=torch.bfloat16) * 3.2)(B, Hkv, Sk, Dh)),
            "v_cache": _w((lambda *s: torch.randn(*s, dtype=torch.bfloat16) * 0.4)(B, Hkv, Sk, Dh)),
            "wq": _w((lambda *s: torch.randn(*s, dtype=torch.bfloat16) * 3.2)(D, Hq * Dh) * (D ** -0.5)),
            "wo": _w((lambda *s: torch.randn(*s, dtype=torch.bfloat16) * 0.4)(Hq * Dh, D) * ((Hq * Dh) ** -0.5)),
        }
    _A_in = _A_setup()

    def _B_setup():
        B, S, D, F = 8, 1, 576, 4608
        return {
            "x": _w((lambda *s: torch.randn(*s, dtype=torch.bfloat16) * 0.05)(B, S, D)),
            "w_gate": _w((lambda *s: torch.randn(*s, dtype=torch.bfloat16) * 0.05)(D, F)),
            "w_up": _w((lambda *s: torch.randn(*s, dtype=torch.bfloat16) * 0.05)(D, F)),
            "w_down": _w((lambda *s: torch.randn(*s, dtype=torch.bfloat16) * 0.05)(F, D)),
        }
    _B_in = _B_setup()

    return {
        "A_x": _A_in["x"],
        "A_k_cache": _A_in["k_cache"],
        "A_v_cache": _A_in["v_cache"],
        "A_wq": _A_in["wq"],
        "A_wo": _A_in["wo"],
        "B_x": _B_in["x"],
        "B_w_gate": _B_in["w_gate"],
        "B_w_up": _B_in["w_up"],
        "B_w_down": _B_in["w_down"],
        "gate_w": _w((lambda *s: torch.randn(*s, dtype=torch.bfloat16) * 0.05)(576) * 16.0),
    }

def reference(A_x, A_k_cache, A_v_cache, A_wq, A_wo, B_x, B_w_gate, B_w_up, B_w_down, gate_w):
    def _A_ref(x, k_cache, v_cache, wq, wo):
        B, Sq, D = x.shape
        Hq, Dh = 32, 128
        Hkv, Sk = k_cache.shape[1], k_cache.shape[2]
        W = min(4096, max(256, Sk // 4))
        rep = Hq // Hkv
        q = (lambda x, *d: x.permute(*d))((x @ wq).reshape(B, Sq, Hq, Dh), 0, 2, 1, 3)
        k = (lambda x, n, axis: torch.repeat_interleave(x, n, dim=axis))(k_cache[:, :, Sk - W:, :], rep, axis=1)
        v = (lambda x, n, axis: torch.repeat_interleave(x, n, dim=axis))(v_cache[:, :, Sk - W:, :], rep, axis=1)
        scores = torch.einsum("bhqd,bhkd->bhqk", q, k) * (Dh ** -0.5)
        p = _softmax(scores, axis=-1)
        o = torch.einsum("bhqk,bhkd->bhqd", p, v)
        return (lambda x, *d: x.permute(*d))(o, 0, 2, 1, 3).reshape(B, Sq, Hq * Dh) @ wo

    def _B_ref(x, w_gate, w_up, w_down):
        g = _silu(x @ w_gate)
        u = x @ w_up
        return (g * u) @ w_down

    x = A_x
    g = _sigmoid((x * gate_w).sum(-1, keepdims=True))
    a = _A_ref(x, A_k_cache, A_v_cache, A_wq, A_wo)
    b = _B_ref(x, B_w_gate, B_w_up, B_w_down)
    return g * a + (1 - g) * b
```

```cpp
#include <hip/hip_runtime.h>
#include <hip/hip_bf16.h>
#include <stdint.h>

typedef unsigned short u16;
__device__ __forceinline__ float bf2f(u16 v) { return __builtin_bit_cast(float, (unsigned)v << 16); }
__device__ __forceinline__ u16 f2bf(float f) { __hip_bfloat16 h = __float2bfloat16(f); return __builtin_bit_cast(u16, h); }

constexpr int B = 8, D = 576, HQ = 32, HKV = 16, SK = 4096, DH = 128, W = 1024, F = 4608;

__global__ void k1(const u16* x, const u16* wq, const u16* wg, const u16* wu, float* q, float* h) {
  __shared__ float xs[B * D];
  for (int i = threadIdx.x; i < B * D; i += blockDim.x) xs[i] = bf2f(x[i]);
  __syncthreads();
  int n = blockIdx.x * blockDim.x + threadIdx.x;
  if (n < HQ * DH) {
    float acc[B] = {0};
    for (int k = 0; k < D; ++k) { float w = bf2f(wq[k * (HQ * DH) + n]);
      for (int b = 0; b < B; ++b) acc[b] += xs[b * D + k] * w; }
    for (int b = 0; b < B; ++b) q[b * (HQ * DH) + n] = bf2f(f2bf(acc[b]));
  } else if (n < HQ * DH + F) {
    int f = n - HQ * DH;
    float ag[B] = {0}, au[B] = {0};
    for (int k = 0; k < D; ++k) { float g = bf2f(wg[k * F + f]), u = bf2f(wu[k * F + f]);
      for (int b = 0; b < B; ++b) { ag[b] += xs[b * D + k] * g; au[b] += xs[b * D + k] * u; } }
    for (int b = 0; b < B; ++b) { float g = ag[b]; float s = g / (1.f + __expf(-g)); h[b * F + f] = bf2f(f2bf(s * au[b])); }
  }
}

__global__ void k2(const u16* kc, const u16* vc, const float* q, float* o) {
  int b = blockIdx.x / HQ, hq = blockIdx.x % HQ, hk = hq / 2;
  __shared__ float qs[DH]; __shared__ float p[W]; __shared__ float red[256];
  int t = threadIdx.x;
  if (t < DH) qs[t] = q[b * HQ * DH + hq * DH + t];
  __syncthreads();
  const u16* kb = kc + ((size_t)(b * HKV + hk) * SK + (SK - W)) * DH;
  const u16* vb = vc + ((size_t)(b * HKV + hk) * SK + (SK - W)) * DH;
  float m = -INFINITY;
  for (int j = t; j < W; j += 256) { float s = 0; for (int d = 0; d < DH; ++d) s += qs[d] * bf2f(kb[j * DH + d]); s *= 0.08838834764831845f; p[j] = s; m = fmaxf(m, s); }
  red[t] = m; __syncthreads();
  for (int s = 128; s > 0; s >>= 1) { if (t < s) red[t] = fmaxf(red[t], red[t + s]); __syncthreads(); }
  m = red[0]; __syncthreads();
  float l = 0; for (int j = t; j < W; j += 256) { float e = __expf(p[j] - m); p[j] = e; l += e; }
  red[t] = l; __syncthreads();
  for (int s = 128; s > 0; s >>= 1) { if (t < s) red[t] += red[t + s]; __syncthreads(); }
  l = red[0]; __syncthreads();
  int d = t & 127, hf = t >> 7; float acc = 0;
  for (int j = hf * 512; j < hf * 512 + 512; ++j) acc += p[j] * bf2f(vb[j * DH + d]);
  red[t] = acc; __syncthreads();
  if (t < 128) o[b * HQ * DH + hq * DH + t] = bf2f(f2bf((red[t] + red[t + 128]) / l));
}

__global__ void k3(const u16* x, const u16* gw, const float* o, const float* h, const u16* wo, const u16* wd, u16* out) {
  int b = blockIdx.x / 9, c = (blockIdx.x % 9) * 64 + (threadIdx.x & 63), g = threadIdx.x >> 6;
  __shared__ float ra[256], rb[256], gs[256];
  float a = 0, bb = 0;
  for (int k = g; k < HQ * DH; k += 4) a += o[b * HQ * DH + k] * bf2f(wo[k * D + c]);
  for (int k = g; k < F; k += 4) bb += h[b * F + k] * bf2f(wd[k * D + c]);
  float s = 0; for (int k = threadIdx.x; k < D; k += 256) s += bf2f(f2bf(bf2f(x[b * D + k]) * bf2f(gw[k])));
  ra[threadIdx.x] = a; rb[threadIdx.x] = bb; gs[threadIdx.x] = s; __syncthreads();
  if (threadIdx.x < 64) {
    float st = 0; for (int i = 0; i < 256; ++i) st += gs[i];
    float gate = 1.f / (1.f + __expf(-st));
    int t = threadIdx.x;
    a = ra[t] + ra[t + 64] + ra[t + 128] + ra[t + 192]; bb = rb[t] + rb[t + 64] + rb[t + 128] + rb[t + 192];
    out[b * D + c] = f2bf(gate * a + (1.f - gate) * bb);
  }
}

extern "C" void kernel_launch(void* const* d_in, const int* in_sizes, int n_in, void* d_out, int out_size, void* d_ws, size_t ws_size, hipStream_t stream) {
  const u16* x = (const u16*)d_in[0]; const u16* kc = (const u16*)d_in[1]; const u16* vc = (const u16*)d_in[2];
  const u16* wq = (const u16*)d_in[3]; const u16* wo = (const u16*)d_in[4];
  const u16* wg = (const u16*)d_in[6]; const u16* wu = (const u16*)d_in[7]; const u16* wd = (const u16*)d_in[8]; const u16* gw = (const u16*)d_in[9];
  float* q = (float*)d_ws; float* h = q + B * HQ * DH; float* o = h + B * F;
  k1<<<(HQ * DH + F) / 64, 64, 0, stream>>>(x, wq, wg, wu, q, h);
  k2<<<B * HQ, 256, 0, stream>>>(kc, vc, q, o);
  k3<<<B * 9, 256, 0, stream>>>(x, gw, o, h, wo, wd, (u16*)d_out);
}
```

```cpp
#include <hip/hip_runtime.h>
#include <stdint.h>

typedef unsigned u32;
typedef unsigned long long u64;
typedef u32 v4u __attribute__((ext_vector_type(4)));
typedef u32 v2u __attribute__((ext_vector_type(2)));
typedef float v4f __attribute__((ext_vector_type(4)));
typedef float v2f __attribute__((ext_vector_type(2)));
typedef short v4s __attribute__((ext_vector_type(4)));
typedef __bf16 v8bf __attribute__((ext_vector_type(8)));
typedef __bf16 v2bf __attribute__((ext_vector_type(2)));
#define LAS __attribute__((address_space(3)))
#define DEVINL __device__ __forceinline__

#define KV_POL ""
#define W_POL ""
#define V_POL KV_POL

constexpr u32 WS_EPOCH = 0;
constexpr u32 WS_GATE = 256;
constexpr u32 WS_LSE = 4096;
constexpr u32 WS_Q = 16384;
constexpr u32 WS_H = WS_Q + 8 * 2048 * 8;
constexpr u32 WS_O = WS_H + 8 * 2304 * 8;
constexpr u32 WS_P = WS_O + 32 * 16 * 512;
constexpr u32 P_ROW = 576 * 64;
constexpr u32 WS_DIAG = 4u << 20;
constexpr u32 L_T1 = 0;
constexpr u32 L_X = 73728;
constexpr u32 L_WO = L_X + 9216;
constexpr u32 L_Q = L_WO + 2 * 16384;
constexpr u32 L_ML = L_Q + 512;
constexpr u32 L_CTL = L_ML + 64;
constexpr u32 L_OS = L_CTL + 64;
constexpr u32 L_END = L_OS + 4096;
constexpr u32 C_S1 = L_CTL + 0, C_Q = L_CTL + 4, C_ML = L_CTL + 8, C_O = L_CTL + 12, C_VI = L_CTL + 16;
constexpr u32 SPIN_CAP = 1u << 17;

struct Params {
  const char* x; const char* k; const char* v; const char* wq; const char* wo; const char* wg; const char* wu; const char* wd;
  const char* gw; char* out; char* ws;
};

DEVINL v4f mfma(v4u a, v4u b, v4f c) {
  return __builtin_amdgcn_mfma_f32_16x16x32_bf16(__builtin_bit_cast(v8bf, a), __builtin_bit_cast(v8bf, b), c, 0, 0, 0);
}
DEVINL u32 pk_bf16(float lo, float hi) { v2f f = {lo, hi}; return __builtin_bit_cast(u32, __builtin_convertvector(f, v2bf)); }
DEVINL float bf16r(float f) { return __builtin_bit_cast(float, pk_bf16(f, f) & 0xffff0000u); }
template <int CTRL> DEVINL float dpp(float x) {
  return __builtin_bit_cast(float, __builtin_amdgcn_mov_dpp(__builtin_bit_cast(int, x), CTRL, 0xf, 0xf, true));
}
constexpr int XOR1 = 0xB1, XOR2 = 0x4E, ROR4 = 0x124, ROR8 = 0x128, HMIR = 0x141;
DEVINL float xg_max(float x) {
  auto s = __builtin_amdgcn_permlane16_swap(__float_as_uint(x), __float_as_uint(x), false, false);
  x = fmaxf(__uint_as_float(s[0]), __uint_as_float(s[1]));
  auto t = __builtin_amdgcn_permlane32_swap(__float_as_uint(x), __float_as_uint(x), false, false);
  return fmaxf(__uint_as_float(t[0]), __uint_as_float(t[1]));
}
DEVINL float xg_sum(float x) {
  auto s = __builtin_amdgcn_permlane16_swap(__float_as_uint(x), __float_as_uint(x), false, false);
  x = __uint_as_float(s[0]) + __uint_as_float(s[1]);
  auto t = __builtin_amdgcn_permlane32_swap(__float_as_uint(x), __float_as_uint(x), false, false);
  return __uint_as_float(t[0]) + __uint_as_float(t[1]);
}
DEVINL void lds_inc(u32 addr) {
  u64 keep; const u32 one = 1;
  asm volatile("s_mov_b64 %0, exec\n\ts_mov_b64 exec, 1\n\tds_add_u32 %1, %2\n\ts_mov_b64 exec, %0" : "=&s"(keep) : "v"(addr), "v"(one) : "memory");
}
DEVINL void lds_wait_ge(u32 addr, u32 want) {
  _Pragma("nounroll") for (u32 spins = 0; spins < (SPIN_CAP << 4); ++spins) {
    u32 r;
    asm volatile("ds_read_b32 %0, %1\n\ts_waitcnt lgkmcnt(0)" : "=v"(r) : "v"(addr) : "memory");
    if ((u32)__builtin_amdgcn_readfirstlane(r) >= want) break;
    __builtin_amdgcn_s_sleep(1);
  }
}
#define DMA16(gptr, ldsdst, POL) do { u32 keep_; \
  asm volatile("s_mov_b32 %0, m0\n\ts_mov_b32 m0, %2\n\ts_nop 0\n\tglobal_load_lds_dwordx4 %1, off" POL "\n\ts_mov_b32 m0, %0" \
               : "=&s"(keep_) : "v"(gptr), "s"(ldsdst) : "memory"); } while (0)
#define VMCNT(n) asm volatile("s_waitcnt vmcnt(" #n ")" ::: "memory")
DEVINL void st16_dev(char* base, u32 off, v4u d) { asm volatile("global_store_dwordx4 %0, %1, %2 sc1\n\ts_nop 1" :: "v"(off), "v"(d), "s"(base) : "memory"); }
DEVINL void st8_dev(char* base, u32 off, v2u d) { asm volatile("global_store_dwordx2 %0, %1, %2 sc1" :: "v"(off), "v"(d), "s"(base) : "memory"); }
DEVINL v4u trfrag(LAS char* l, u32 a0, u32 imm) {
  const v2u x = __builtin_bit_cast(v2u, __builtin_amdgcn_ds_read_tr16_b64_v4i16((LAS v4s*)(l + a0 + imm)));
  const v2u y = __builtin_bit_cast(v2u, __builtin_amdgcn_ds_read_tr16_b64_v4i16((LAS v4s*)(l + a0 + imm + 2048)));
  return v4u{x.x, x.y, y.x, y.y};
}
DEVINL u32 tr_lane_addr(int lane, int j) {
  const int r0 = (lane >> 2) & 15, p = lane & 3;
  const int c = (p < 2 ? j : j + 4) ^ ((r0 >> 1) & 3);
  return r0 * 128 + c * 16 + 8 * (p & 1);
}

#define STAMP(i)
#define STAMP_DECL
#define STAMP_FLUSH()

__global__ void __launch_bounds__(512, 2) fused_decode(Params p) {
  __shared__ __attribute__((aligned(1024))) char lds_raw[L_END];
  LAS char* lds = (LAS char*)lds_raw;
  const u32 lbase = (u32)(uintptr_t)lds;
  const int tid = threadIdx.x, lane = tid & 63, wave = __builtin_amdgcn_readfirstlane(tid >> 6);
  const int bid = blockIdx.x, xc = bid & 7, jc = bid >> 3;
  const int hk = 2 * xc + (jc & 1), half = (jc >> 1) & 1, bat = jc >> 2;
  const int g = lane >> 4, n = lane & 15;
  STAMP_DECL;
  STAMP(0);
  u32 ep;
  { const u32 z = 0; asm volatile("global_load_dword %0, %1, %2 sc1" : "=v"(ep) : "v"(z), "s"(p.ws) : "memory"); }

  if (wave < 4) {
    const size_t reg = ((size_t)(bat * 16 + hk) * 4096 + 3072 + half * 512 + wave * 128) * 256;
    const char* kb = p.k + reg;
    const char* vb = p.v + reg;
    const u32 rot = (((u32)(bid * 4 + wave) * 2654435761u) >> 20) & 7;
    const u32 koff = n * 256 + g * 16, voff = g * 1024 + n * 16;
    v4u K[32], V[32];
#define LDK(t) do { const char* b_ = kb + (((t) + rot) & 7) * 4096; \
    asm volatile("global_load_dwordx4 %0, %4, %5" KV_POL "\n\tglobal_load_dwordx4 %1, %4, %5 offset:64" KV_POL "\n\t" \
                 "global_load_dwordx4 %2, %4, %5 offset:128" KV_POL "\n\tglobal_load_dwordx4 %3, %4, %5 offset:192" KV_POL \
                 : "=&v"(K[4 * (t)]), "=&v"(K[4 * (t) + 1]), "=&v"(K[4 * (t) + 2]), "=&v"(K[4 * (t) + 3]) : "v"(koff), "s"(b_) : "memory"); } while (0)
#define LDV(t) do { const char* b_ = vb + (((t) + rot) & 7) * 4096; \
    asm volatile("global_load_dwordx4 %0, %4, %5" V_POL "\n\tglobal_load_dwordx4 %1, %4, %5 offset:256" V_POL "\n\t" \
                 "global_load_dwordx4 %2, %4, %5 offset:512" V_POL "\n\tglobal_load_dwordx4 %3, %4, %5 offset:768" V_POL \
                 : "=&v"(V[4 * (t)]), "=&v"(V[4 * (t) + 1]), "=&v"(V[4 * (t) + 2]), "=&v"(V[4 * (t) + 3]) : "v"(voff), "s"(b_) : "memory"); } while (0)
#define TIE4(A, t) asm volatile("" : "+v"(A[4 * (t)]), "+v"(A[4 * (t) + 1]), "+v"(A[4 * (t) + 2]), "+v"(A[4 * (t) + 3]))
    LDK(0); LDK(1); LDK(2);
    u32 T;
    asm volatile("s_waitcnt vmcnt(12)\n\tv_readfirstlane_b32 %0, %1\n\ts_nop 1" : "=s"(T) : "v"(ep) : "memory");
    T += 1u;
    __builtin_amdgcn_s_barrier();
    asm volatile("" ::: "memory");
    STAMP(1);
    LDK(3); LDK(4); LDK(5); LDK(6); LDK(7);
    LDV(0); LDV(1); LDV(2); LDV(3);
    STAMP(2);
    lds_wait_ge(lbase + C_Q, 1);
    STAMP(3);
    v4u qf[4];
#pragma unroll
    for (int ks = 0; ks < 4; ++ks) qf[ks] = *(LAS v4u*)(lds + L_Q + ((n >> 3) & 1) * 256 + ks * 64 + g * 16);
    v4f s[8];
    VMCNT(32);
    TIE4(K, 0); TIE4(K, 1); TIE4(K, 2); TIE4(K, 3);
#pragma unroll
    for (int t = 0; t < 4; ++t) {
      s[t] = v4f{0.f, 0.f, 0.f, 0.f};
#pragma unroll
      for (int ks = 0; ks < 4; ++ks) s[t] = mfma(K[4 * t + ks], qf[ks], s[t]);
    }
    asm volatile("s_nop 7\n\ts_nop 1" : "+v"(s[0]), "+v"(s[1]), "+v"(s[2]), "+v"(s[3]));
    LDV(4); LDV(5); LDV(6); LDV(7);
    lds_inc(lbase + C_VI);
    STAMP(4);
    VMCNT(32);
    TIE4(K, 4); TIE4(K, 5); TIE4(K, 6); TIE4(K, 7);
#pragma unroll
    for (int t = 4; t < 8; ++t) {
      s[t] = v4f{0.f, 0.f, 0.f, 0.f};
#pragma unroll
      for (int ks = 0; ks < 4; ++ks) s[t] = mfma(K[4 * t + ks], qf[ks], s[t]);
    }
    constexpr float C2 = 0.08838834764831845f * 1.4426950408889634f;
    float mx = s[0][0];
#pragma unroll
    for (int t = 0; t < 8; ++t)
#pragma unroll
      for (int r = 0; r < 4; ++r) mx = fmaxf(mx, s[t][r]);
    mx = xg_max(mx) * C2;
    float ls = 0.f;
#pragma unroll
    for (int t = 0; t < 8; ++t)
#pragma unroll
      for (int r = 0; r < 4; ++r) { s[t][r] = __builtin_amdgcn_exp2f(fmaf(s[t][r], C2, -mx)); ls += s[t][r]; }
    ls = xg_sum(ls);
    if ((lane & 0x37) == 0) *(LAS v2f*)(lds + L_ML + wave * 16 + (lane >> 3) * 8) = v2f{mx, ls};
    lds_inc(lbase + C_ML);
    u32 msk[4];
#pragma unroll
    for (int i = 0; i < 4; ++i) msk[i] = (((lane & 7) >> 1) == i) ? ((lane & 1) ? 0xffff0000u : 0x0000ffffu) : 0u;
    v4f acc = {0.f, 0.f, 0.f, 0.f};
    VMCNT(16);
    TIE4(V, 0); TIE4(V, 1); TIE4(V, 2); TIE4(V, 3);
#pragma unroll
    for (int i = 0; i < 16; ++i) {
      const u32 pp = pk_bf16(s[i >> 2][i & 3], s[i >> 2][i & 3]);
      acc = mfma(V[i], v4u{pp & msk[0], pp & msk[1], pp & msk[2], pp & msk[3]}, acc);
    }
    v4u pb[16];
#pragma unroll
    for (int i = 0; i < 16; ++i) {
      const u32 pp = pk_bf16(s[4 + (i >> 2)][i & 3], s[4 + (i >> 2)][i & 3]);
      pb[i] = v4u{pp & msk[0], pp & msk[1], pp & msk[2], pp & msk[3]};
    }
    lds_wait_ge(lbase + C_ML, 4);
    float fw;
    {
      v2f ml[4];
#pragma unroll
      for (int w = 0; w < 4; ++w) ml[w] = *(LAS v2f*)(lds + L_ML + w * 16 + ((n >> 3) & 1) * 8);
      const float M = fmaxf(fmaxf(ml[0].x, ml[1].x), fmaxf(ml[2].x, ml[3].x));
      float L = 0.f;
#pragma unroll
      for (int w = 0; w < 4; ++w) L = fmaf(ml[w].y, __builtin_amdgcn_exp2f(ml[w].x - M), L);
      fw = __builtin_amdgcn_exp2f(mx - M) / L;
    }
    TIE4(pb, 0); TIE4(pb, 1); TIE4(pb, 2); TIE4(pb, 3);
    asm volatile("" : "+v"(fw), "+v"(acc));
    STAMP(5);
    VMCNT(12); TIE4(V, 4);
#pragma unroll
    for (int i = 0; i < 4; ++i) acc = mfma(V[16 + i], pb[i], acc);
    asm volatile("" : "+v"(acc));
    VMCNT(8); TIE4(V, 5);
#pragma unroll
    for (int i = 4; i < 8; ++i) acc = mfma(V[16 + i], pb[i], acc);
    asm volatile("" : "+v"(acc));
    VMCNT(4); TIE4(V, 6);
#pragma unroll
    for (int i = 8; i < 12; ++i) acc = mfma(V[16 + i], pb[i], acc);
    asm volatile("" : "+v"(acc));
    VMCNT(2); asm volatile("" : "+v"(V[28]), "+v"(V[29]));
    acc = mfma(V[28], pb[12], acc); acc = mfma(V[29], pb[13], acc);
    asm volatile("" : "+v"(acc));
    VMCNT(0); asm volatile("" : "+v"(V[30]), "+v"(V[31]));
    acc = mfma(V[30], pb[14], acc); acc = mfma(V[31], pb[15], acc);
    STAMP(6);
    acc *= fw;
    {
      const u32 a_own = lbase + L_OS + wave * 1024 + lane * 16, a_all = lbase + L_OS + lane * 16, a_cnt = lbase + C_O, one = 1;
      u64 keep; u32 tk; v4f o0, o1, o2, o3;
      asm volatile("ds_write_b128 %6, %7\n\t"
                   "s_mov_b64 %0, exec\n\ts_mov_b64 exec, 1\n\tds_add_rtn_u32 %1, %8, %9\n\ts_mov_b64 exec, %0\n\t"
                   "ds_read_b128 %2, %10\n\tds_read_b128 %3, %10 offset:1024\n\tds_read_b128 %4, %10 offset:2048\n\tds_read_b128 %5, %10 offset:3072\n\t"
                   "s_waitcnt lgkmcnt(0)"
                   : "=&s"(keep), "=&v"(tk), "=&v"(o0), "=&v"(o1), "=&v"(o2), "=&v"(o3)
                   : "v"(a_own), "v"(acc), "v"(a_cnt), "v"(one), "v"(a_all) : "memory");
      if (__builtin_amdgcn_readfirstlane(tk) == 3) {
        const v4f o = (o0 + o1) + (o2 + o3);
        const u32 off = WS_O + (2 * hk + ((n >> 3) & 1)) * 8192 + (2 * bat + half) * 512 + (8 * g + (n & 7)) * 16;
        st16_dev(p.ws, off, v4u{pk_bf16(o[0], o[1]), T, pk_bf16(o[2], o[3]), T});
      }
    }
    STAMP(7);
    if (wave == 0 || (wave == 1 && bid < 32)) {
      const int tau = wave == 0 ? bid : 256 + bid;
      const u32 po = WS_P + (lane >> 3) * P_ROW + tau * 128 + (lane & 7) * 16;
      const u32 go = WS_GATE + (lane & 3) * 16;
      const u32 po1 = po + 8 * P_ROW, po2 = po + 16 * P_ROW, po3 = po + 24 * P_ROW, po4 = po + 32 * P_ROW, po5 = po + 40 * P_ROW;
      bool first = true;
      _Pragma("nounroll") for (u32 spins = 0; spins < SPIN_CAP; ++spins) {
        v4u a0, a1, a2, a3, a4, a5, gg;
        asm volatile("global_load_dwordx4 %0, %7, %14 sc1\n\tglobal_load_dwordx4 %1, %8, %14 sc1\n\tglobal_load_dwordx4 %2, %9, %14 sc1\n\t"
                     "global_load_dwordx4 %3, %10, %14 sc1\n\tglobal_load_dwordx4 %4, %11, %14 sc1\n\tglobal_load_dwordx4 %5, %12, %14 sc1\n\t"
                     "global_load_dwordx4 %6, %13, %14 sc1\n\ts_waitcnt vmcnt(0)"
                     : "=&v"(a0), "=&v"(a1), "=&v"(a2), "=&v"(a3), "=&v"(a4), "=&v"(a5), "=&v"(gg)
                     : "v"(po), "v"(po1), "v"(po2), "v"(po3), "v"(po4), "v"(po5), "v"(go), "s"(p.ws) : "memory");
        const u32 bad = (a0.y ^ T) | (a0.w ^ T) | (a1.y ^ T) | (a1.w ^ T) | (a2.y ^ T) | (a2.w ^ T) | (a3.y ^ T) | (a3.w ^ T) |
                        (a4.y ^ T) | (a4.w ^ T) | (a5.y ^ T) | (a5.w ^ T) | (gg.y ^ T) | (gg.w ^ T);
        const bool ok = __builtin_amdgcn_ballot_w64(bad != 0) == 0;
        if (ok || first) {
          first = false;
#define F_(v) __uint_as_float(v)
          const float g0 = F_(gg.x), g1 = F_(gg.z);
          float v0 = g0 * ((F_(a0.x) + F_(a1.x)) + (F_(a2.x) + F_(a3.x))) + (1.f - g0) * (F_(a4.x) + F_(a5.x));
          float v1 = g1 * ((F_(a0.z) + F_(a1.z)) + (F_(a2.z) + F_(a3.z))) + (1.f - g1) * (F_(a4.z) + F_(a5.z));
          v0 += dpp<ROR8>(v0); v1 += dpp<ROR8>(v1);
          v0 = xg_sum(v0); v1 = xg_sum(v1);
          const float w0 = dpp<ROR4>(v0), w1 = dpp<ROR4>(v1);
          const bool hi = (lane & 4) != 0;
          const u32 r = hi ? pk_bf16(w1, v1) : pk_bf16(v0, w0);
          if (ok) {
            if (lane < 8) *(u32*)(p.out + ((2 * (lane & 3) + (hi ? 1 : 0)) * 576 + 2 * tau) * 2) = r;
            if (tau == 0 && lane == 0) __hip_atomic_store((u32*)(p.ws + WS_EPOCH), T, __ATOMIC_RELAXED, __HIP_MEMORY_SCOPE_AGENT);
            break;
          }
          asm volatile("" :: "v"(r));
        }
        __builtin_amdgcn_s_sleep(1);
      }
      STAMP(8);
    }
    STAMP_FLUSH();
    return;
  }

  const int hw = wave - 4;
  __builtin_amdgcn_s_setprio(3);
  const int drow = lane >> 3, dch = (lane & 7) ^ ((drow >> 1) & 3);
  const bool isS = jc >= 26, isQ = jc >= 18 && !isS;
  const int unit = isS ? xc * 6 + (jc - 26) : isQ ? xc * 8 + (jc - 18) : xc * 18 + jc;
  const char* t1src = nullptr; size_t t1step = 0;
  if (!isS) {
#pragma unroll
    for (int i = 0; i < 3; ++i) { const int c = min(hw + 4 * i, 8); DMA16(p.x + c * 1024 + lane * 16, lbase + L_X + c * 1024, ""); }
    if (isQ) { t1src = p.wq + (size_t)(144 * hw + drow) * 8192 + unit * 128 + dch * 16; t1step = 8 * 8192; }
    else { t1src = (dch < 4 ? p.wg : p.wu) + (size_t)(144 * hw + drow) * 9216 + unit * 64 + (dch & 3) * 16; t1step = 8 * 9216; }
#pragma unroll
    for (int i = 0; i < 8; ++i) { DMA16(t1src, lbase + L_T1 + (18 * hw + i) * 1024, W_POL); t1src += t1step; }
    VMCNT(11);
  } else {
    VMCNT(0);
  }
  u32 T;
  asm volatile("v_readfirstlane_b32 %0, %1\n\ts_nop 1" : "=s"(T) : "v"(ep) : "memory");
  T += 1u;
  if (hw == 0 && lane < 16) *(LAS u32*)(lds + L_CTL + lane * 4) = 0u;
  asm volatile("s_waitcnt lgkmcnt(0)" ::: "memory");
  __builtin_amdgcn_s_barrier();
  asm volatile("" ::: "memory");
  STAMP(1);
  const int beta = 2 * (n >> 2) + (n & 1);
  if (!isS) {
#pragma unroll
    for (int i = 8; i < 18; ++i) { DMA16(t1src, lbase + L_T1 + (18 * hw + i) * 1024, W_POL); t1src += t1step; }
    VMCNT(0);
    lds_inc(lbase + C_S1);
    lds_wait_ge(lbase + C_S1, 4);
    STAMP(2);
    const u32 ta = L_T1 + tr_lane_addr(lane, hw);
    const u32 xa = L_X + beta * 1152 + g * 8;
    v4f acc = {0.f, 0.f, 0.f, 0.f};
#pragma unroll
    for (int ks = 0; ks < 18; ++ks) {
      const v2u x0 = *(LAS v2u*)(lds + xa + ks * 64), x1 = *(LAS v2u*)(lds + xa + ks * 64 + 32);
      acc = mfma(v4u{x0.x, x0.y, x1.x, x1.y}, trfrag(lds, ta, ks * 4096), acc);
    }
    if (isQ) {
      const float n0 = dpp<XOR1>(acc[0]), n1 = dpp<XOR1>(acc[1]);
      const u32 pk = (n & 1) ? pk_bf16(n1, acc[1]) : pk_bf16(acc[0], n0);
      const int col = 64 * unit + (n >> 3) * 32 + 8 * hw + (n & 7);
      st8_dev(p.ws, WS_Q + ((2 * g + (n & 1)) * 2048 + (col >> 1)) * 8, v2u{pk, T});
    } else {
      const float o0 = dpp<ROR8>(acc[0]), o1 = dpp<ROR8>(acc[1]);
      const float gv = bf16r(n < 8 ? acc[0] : o1), uv = bf16r(n < 8 ? o0 : acc[1]);
      const float sg = bf16r(gv / (1.f + __expf(-gv)));
      const float hv = sg * uv, hn = dpp<XOR1>(hv);
      const int col = 32 * unit + 8 * hw + (n & 7);
      if ((n & 1) == 0) st8_dev(p.ws, WS_H + ((2 * g + (n >> 3)) * 2304 + (col >> 1)) * 8, v2u{pk_bf16(hv, hn), T});
    }
    STAMP(3);
  }

  if (hw == 3) {
    if (isS && unit == 0) {
      const v4u* xp = (const v4u*)(p.x + (lane >> 3) * 1152) + (lane & 7);
      const v4u* gp = (const v4u*)p.gw + (lane & 7);
      float sum = 0.f;
#pragma unroll
      for (int i = 0; i < 9; ++i) {
        const v4u a = xp[8 * i], b = gp[8 * i];
#pragma unroll
        for (int e = 0; e < 4; ++e) {
          sum = fmaf(__uint_as_float(a[e] << 16), __uint_as_float(b[e] << 16), sum);
          sum = fmaf(__uint_as_float(a[e] & 0xffff0000u), __uint_as_float(b[e] & 0xffff0000u), sum);
        }
      }
      sum += dpp<XOR1>(sum); sum += dpp<XOR2>(sum); sum += dpp<HMIR>(sum);
      const float gt = 1.f / (1.f + __expf(-sum));
      if ((lane & 7) == 0) st8_dev(p.ws, WS_GATE + (lane >> 3) * 8, v2u{__float_as_uint(gt), T});
    }
    const u32 qo = WS_Q + (bat * 2048 + hk * 128) * 8 + lane * 16;
    v4u qv;
    _Pragma("nounroll") for (u32 spins = 0; spins < SPIN_CAP; ++spins) {
      asm volatile("global_load_dwordx4 %0, %1, %2 sc1\n\ts_waitcnt vmcnt(0)" : "=&v"(qv) : "v"(qo), "s"(p.ws) : "memory");
      if (__builtin_amdgcn_ballot_w64(((qv.y ^ T) | (qv.w ^ T)) != 0) == 0) break;
      __builtin_amdgcn_s_sleep(4);
    }
    *(LAS v2u*)(lds + L_Q + lane * 8) = v2u{qv.x, qv.z};
    lds_inc(lbase + C_Q);
    STAMP(4);
    lds_wait_ge(lbase + C_ML, 4);
    if (lane < 2) {
      v2f ml[4];
#pragma unroll
      for (int w = 0; w < 4; ++w) ml[w] = *(LAS v2f*)(lds + L_ML + w * 16 + lane * 8);
      const float M = fmaxf(fmaxf(ml[0].x, ml[1].x), fmaxf(ml[2].x, ml[3].x));
      float L = 0.f;
#pragma unroll
      for (int w = 0; w < 4; ++w) L = fmaf(ml[w].y, __builtin_amdgcn_exp2f(ml[w].x - M), L);
      st8_dev(p.ws, WS_LSE + ((2 * hk + lane) * 16 + 2 * bat + half) * 8, v2u{__float_as_uint(M + __builtin_amdgcn_logf(L)), T});
    }
    STAMP(5);
    STAMP_FLUSH();
    return;
  }

  const u32 pst = ((n & 7) + (n >> 3) * 32) * 64 + g * 16;
  if (isS) {
    const int rg = unit / 3, cg = 3 * (unit % 3) + hw;
    v4u A[9];
    {
      const u32 ho = WS_H + (beta * 2304 + rg * 144) * 8 + g * 16;
      _Pragma("nounroll") for (u32 spins = 0; spins < SPIN_CAP; ++spins) {
        v4u h[18];
#define LH(i) "global_load_dwordx4 %" #i ", %18, %19 offset:" #i "*64 sc1\n\t"
        asm volatile(LH(0) LH(1) LH(2) LH(3) LH(4) LH(5) LH(6) LH(7) LH(8) LH(9) LH(10) LH(11) LH(12) LH(13) LH(14) LH(15) LH(16) LH(17) "s_waitcnt vmcnt(0)"
                     : "=&v"(h[0]), "=&v"(h[1]), "=&v"(h[2]), "=&v"(h[3]), "=&v"(h[4]), "=&v"(h[5]), "=&v"(h[6]), "=&v"(h[7]), "=&v"(h[8]),
                       "=&v"(h[9]), "=&v"(h[10]), "=&v"(h[11]), "=&v"(h[12]), "=&v"(h[13]), "=&v"(h[14]), "=&v"(h[15]), "=&v"(h[16]), "=&v"(h[17])
                     : "v"(ho), "s"(p.ws) : "memory");
        u32 bad = 0;
#pragma unroll
        for (int i = 0; i < 18; ++i) bad |= (h[i].y ^ T) | (h[i].w ^ T);
#pragma unroll
        for (int ks = 0; ks < 9; ++ks) A[ks] = v4u{h[2 * ks].x, h[2 * ks].z, h[2 * ks + 1].x, h[2 * ks + 1].z};
        if (__builtin_amdgcn_ballot_w64(bad != 0) == 0) break;
        __builtin_amdgcn_s_sleep(32);
      }
    }
    STAMP(4);
    lds_wait_ge(lbase + C_VI, 4);
    STAMP(5);
    const char* src = p.wd + (size_t)(288 * rg + drow) * 1152 + cg * 128 + dch * 16;
    const u32 tb = L_T1 + hw * 36864;
#pragma unroll
    for (int i = 0; i < 36; ++i) { DMA16(src, lbase + tb + i * 1024, W_POL); src += 8 * 1152; }
    STAMP(6);
    u32 ta[4];
    v4f acc[4];
#pragma unroll
    for (int j = 0; j < 4; ++j) { ta[j] = tb + tr_lane_addr(lane, j); acc[j] = v4f{0.f, 0.f, 0.f, 0.f}; }
#define DSTEP(ks, N) do { VMCNT(N); _Pragma("unroll") for (int j = 0; j < 4; ++j) acc[j] = mfma(A[ks], trfrag(lds, ta[j], (ks) * 4096), acc[j]); } while (0)
    DSTEP(0, 32); DSTEP(1, 28); DSTEP(2, 24); DSTEP(3, 20); DSTEP(4, 16); DSTEP(5, 12); DSTEP(6, 8); DSTEP(7, 4); DSTEP(8, 0);
    const u32 po = WS_P + (32 + rg) * P_ROW + cg * 4096 + pst;
#pragma unroll
    for (int j = 0; j < 4; ++j) st16_dev(p.ws, po + j * 512, v4u{__float_as_uint(acc[j][0]), T, __float_as_uint(acc[j][1]), T});
    STAMP(8);
    STAMP_FLUSH();
    return;
  }
  if (hw == 0 || (hw == 1 && jc < 10)) {
    const int task = hw == 0 ? jc : 26 + jc, head = 4 * xc + task / 9, cg = task % 9;
    lds_wait_ge(lbase + C_VI, 4);
    STAMP(4);
    const char* src = p.wo + (size_t)(128 * head + 8 * (drow & 3) + (drow >> 2)) * 1152 + cg * 128 + dch * 16;
    const u32 tb = L_WO + hw * 16384;
#pragma unroll
    for (int i = 0; i < 16; ++i) { const char* s_ = src + (32 * (i >> 2) + 2 * (i & 3)) * 1152; DMA16(s_, lbase + tb + i * 1024, W_POL); }
    float w0, w1, w2, w3;
    {
      const u32 lo = WS_LSE + (head * 16 + 4 * g) * 8;
      v4u e0, e1;
      _Pragma("nounroll") for (u32 spins = 0; spins < SPIN_CAP; ++spins) {
        asm volatile("global_load_dwordx4 %0, %2, %3 sc1\n\tglobal_load_dwordx4 %1, %2, %3 offset:16 sc1\n\ts_waitcnt vmcnt(0)"
                     : "=&v"(e0), "=&v"(e1) : "v"(lo), "s"(p.ws) : "memory");
        if (__builtin_amdgcn_ballot_w64(((e0.y ^ T) | (e0.w ^ T) | (e1.y ^ T) | (e1.w ^ T)) != 0) == 0) break;
        __builtin_amdgcn_s_sleep(2);
      }
      const float d0 = __uint_as_float(e0.z) - __uint_as_float(e0.x), d1 = __uint_as_float(e1.z) - __uint_as_float(e1.x);
      w0 = 1.f / (1.f + __builtin_amdgcn_exp2f(d0)); w1 = 1.f / (1.f + __builtin_amdgcn_exp2f(-d0));
      w2 = 1.f / (1.f + __builtin_amdgcn_exp2f(d1)); w3 = 1.f / (1.f + __builtin_amdgcn_exp2f(-d1));
    }
    STAMP(5);
    v4u B[16];
#pragma unroll
    for (int j = 0; j < 4; ++j) {
      const u32 ta = tb + tr_lane_addr(lane, j);
#pragma unroll
      for (int ks = 0; ks < 4; ++ks) B[4 * j + ks] = trfrag(lds, ta, ks * 4096);
    }
    TIE4(B, 0); TIE4(B, 1); TIE4(B, 2); TIE4(B, 3);
    STAMP(6);
    const u32 oo = WS_O + head * 8192 + n * 512 + g * 16;
    const u32 po = WS_P + head * P_ROW + cg * 4096 + pst;
    bool first = true;
    _Pragma("nounroll") for (u32 spins = 0; spins < SPIN_CAP; ++spins) {
      v4u u[8];
#define LO(i) "global_load_dwordx4 %" #i ", %8, %9 offset:" #i "*64 sc1\n\t"
      asm volatile(LO(0) LO(1) LO(2) LO(3) LO(4) LO(5) LO(6) LO(7) "s_waitcnt vmcnt(0)"
                   : "=&v"(u[0]), "=&v"(u[1]), "=&v"(u[2]), "=&v"(u[3]), "=&v"(u[4]), "=&v"(u[5]), "=&v"(u[6]), "=&v"(u[7])
                   : "v"(oo), "s"(p.ws) : "memory");
      u32 bad = 0;
#pragma unroll
      for (int i = 0; i < 8; ++i) bad |= (u[i].y ^ T) | (u[i].w ^ T);
      const bool ok = __builtin_amdgcn_ballot_w64(bad != 0) == 0;
      if (ok || first) {
        first = false;
        v4f acc[4];
#pragma unroll
        for (int j = 0; j < 4; ++j) acc[j] = v4f{0.f, 0.f, 0.f, 0.f};
#pragma unroll
        for (int ks = 0; ks < 4; ++ks) {
          const v4u A = {u[2 * ks].x, u[2 * ks].z, u[2 * ks + 1].x, u[2 * ks + 1].z};
#pragma unroll
          for (int j = 0; j < 4; ++j) acc[j] = mfma(A, B[4 * j + ks], acc[j]);
        }
        v4u r[4];
#pragma unroll
        for (int j = 0; j < 4; ++j)
          r[j] = v4u{__float_as_uint(fmaf(w0, acc[j][0], w1 * acc[j][1])), T, __float_as_uint(fmaf(w2, acc[j][2], w3 * acc[j][3])), T};
        if (ok) {
#pragma unroll
          for (int j = 0; j < 4; ++j) st16_dev(p.ws, po + j * 512, r[j]);
          break;
        }
        asm volatile("" :: "v"(r[0]), "v"(r[1]), "v"(r[2]), "v"(r[3]));
      }
      __builtin_amdgcn_s_sleep(1);
    }
    STAMP(8);
  }
  STAMP_FLUSH();
}

extern "C" void kernel_launch(void* const* d_in, const int* in_sizes, int n_in, void* d_out, int out_size, void* d_ws, size_t ws_size,
                              hipStream_t stream) {
  Params p;
  p.x = (const char*)d_in[0]; p.k = (const char*)d_in[1]; p.v = (const char*)d_in[2]; p.wq = (const char*)d_in[3]; p.wo = (const char*)d_in[4];
  p.wg = (const char*)d_in[6]; p.wu = (const char*)d_in[7]; p.wd = (const char*)d_in[8]; p.gw = (const char*)d_in[9];
  p.out = (char*)d_out; p.ws = (char*)d_ws;
  hipLaunchKernelGGL(fused_decode, dim3(256), dim3(512), 0, stream, p);
}
```

```cpp
#define PROBE_ST -1
#define PROBE_COND 1
#define PROBE_SIGN 1
#include <hip/hip_runtime.h>
#include <stdint.h>

typedef unsigned u32;
typedef unsigned long long u64;
typedef u32 v4u __attribute__((ext_vector_type(4)));
typedef u32 v2u __attribute__((ext_vector_type(2)));
typedef float v4f __attribute__((ext_vector_type(4)));
typedef float v2f __attribute__((ext_vector_type(2)));
typedef short v4s __attribute__((ext_vector_type(4)));
typedef __bf16 v8bf __attribute__((ext_vector_type(8)));
typedef __bf16 v2bf __attribute__((ext_vector_type(2)));
#define LAS __attribute__((address_space(3)))
#define DEVINL __device__ __forceinline__

#define KV_POL " nt"
#define V_POL KV_POL
#define W_POL " nt"

constexpr u32 WS_EPOCH = 0;
constexpr u32 WS_GATE = 256;
constexpr u32 WS_LSE = 4096;
constexpr u32 WS_Q = 16384;
constexpr u32 WS_H = WS_Q + 8 * 2048 * 8;
constexpr u32 WS_O = WS_H + 8 * 2304 * 8;
constexpr u32 WS_P = WS_O + 32 * 16 * 512;
constexpr u32 P_ROW = 576 * 64;
constexpr u32 WS_DIAG = 4u << 20;
constexpr u32 L_T1 = 0;
constexpr u32 L_X = 73728;
constexpr u32 L_WO = L_X + 9216;
constexpr u32 L_Q = L_WO + 2 * 16384;
constexpr u32 L_ML = L_Q + 512;
constexpr u32 L_CTL = L_ML + 64;
constexpr u32 L_OS = L_CTL + 64;
constexpr u32 L_END = L_OS + 4096;
constexpr u32 C_S1 = L_CTL + 0, C_Q = L_CTL + 4, C_ML = L_CTL + 8, C_O = L_CTL + 12, C_VI = L_CTL + 16;
constexpr u32 SPIN_CAP = 1u << 17;

struct Params {
  const char* x; const char* k; const char* v; const char* wq; const char* wo; const char* wg; const char* wu; const char* wd;
  const char* gw; char* out; char* ws;
};

DEVINL v4f mfma(v4u a, v4u b, v4f c) {
  return __builtin_amdgcn_mfma_f32_16x16x32_bf16(__builtin_bit_cast(v8bf, a), __builtin_bit_cast(v8bf, b), c, 0, 0, 0);
}
DEVINL u32 pk_bf16(float lo, float hi) { v2f f = {lo, hi}; return __builtin_bit_cast(u32, __builtin_convertvector(f, v2bf)); }
DEVINL float bf16r(float f) { return __builtin_bit_cast(float, pk_bf16(f, f) & 0xffff0000u); }
template <int CTRL> DEVINL float dpp(float x) {
  return __builtin_bit_cast(float, __builtin_amdgcn_mov_dpp(__builtin_bit_cast(int, x), CTRL, 0xf, 0xf, true));
}
constexpr int XOR1 = 0xB1, XOR2 = 0x4E, ROR4 = 0x124, ROR8 = 0x128, HMIR = 0x141;
DEVINL float xg_max(float x) {
  auto s = __builtin_amdgcn_permlane16_swap(__float_as_uint(x), __float_as_uint(x), false, false);
  x = fmaxf(__uint_as_float(s[0]), __uint_as_float(s[1]));
  auto t = __builtin_amdgcn_permlane32_swap(__float_as_uint(x), __float_as_uint(x), false, false);
  return fmaxf(__uint_as_float(t[0]), __uint_as_float(t[1]));
}
DEVINL float xg_sum(float x) {
  auto s = __builtin_amdgcn_permlane16_swap(__float_as_uint(x), __float_as_uint(x), false, false);
  x = __uint_as_float(s[0]) + __uint_as_float(s[1]);
  auto t = __builtin_amdgcn_permlane32_swap(__float_as_uint(x), __float_as_uint(x), false, false);
  return __uint_as_float(t[0]) + __uint_as_float(t[1]);
}
DEVINL void lds_inc(u32 addr) {
  u64 keep; const u32 one = 1;
  asm volatile("s_mov_b64 %0, exec\n\ts_mov_b64 exec, 1\n\tds_add_u32 %1, %2\n\ts_mov_b64 exec, %0" : "=&s"(keep) : "v"(addr), "v"(one) : "memory");
}
DEVINL void lds_wait_ge(u32 addr, u32 want) {
  _Pragma("nounroll") for (u32 spins = 0; spins < (SPIN_CAP << 4); ++spins) {
    u32 r;
    asm volatile("ds_read_b32 %0, %1\n\ts_waitcnt lgkmcnt(0)" : "=v"(r) : "v"(addr) : "memory");
    if ((u32)__builtin_amdgcn_readfirstlane(r) >= want) break;
    __builtin_amdgcn_s_sleep(1);
  }
}
#define DMA16(gptr, ldsdst, POL) do { u32 keep_; \
  asm volatile("s_mov_b32 %0, m0\n\ts_mov_b32 m0, %2\n\ts_nop 0\n\tglobal_load_lds_dwordx4 %1, off" POL "\n\ts_mov_b32 m0, %0" \
               : "=&s"(keep_) : "v"(gptr), "s"(ldsdst) : "memory"); } while (0)
#define VMCNT(n) asm volatile("s_waitcnt vmcnt(" #n ")" ::: "memory")
DEVINL void st16_dev(char* base, u32 off, v4u d) { asm volatile("global_store_dwordx4 %0, %1, %2 sc1\n\ts_nop 1" :: "v"(off), "v"(d), "s"(base) : "memory"); }
DEVINL void st8_dev(char* base, u32 off, v2u d) { asm volatile("global_store_dwordx2 %0, %1, %2 sc1" :: "v"(off), "v"(d), "s"(base) : "memory"); }
DEVINL v4u trfrag(LAS char* l, u32 a0, u32 imm) {
  const v2u x = __builtin_bit_cast(v2u, __builtin_amdgcn_ds_read_tr16_b64_v4i16((LAS v4s*)(l + a0 + imm)));
  const v2u y = __builtin_bit_cast(v2u, __builtin_amdgcn_ds_read_tr16_b64_v4i16((LAS v4s*)(l + a0 + imm + 2048)));
  return v4u{x.x, x.y, y.x, y.y};
}
DEVINL u32 tr_lane_addr(int lane, int j) {
  const int r0 = (lane >> 2) & 15, p = lane & 3;
  const int c = (p < 2 ? j : j + 4) ^ ((r0 >> 1) & 3);
  return r0 * 128 + c * 16 + 8 * (p & 1);
}

#define STAMP(i) do { stamps[i] = (u32)__builtin_amdgcn_s_memrealtime(); } while (0)
#define STAMP_DECL u32 stamps[12] = {0, 0, 0, 0, 0, 0, 0, 0, 0, 0, 0, 0}; const u32 cyc0 = (u32)__builtin_amdgcn_s_memtime()
#define STAMP_FLUSH() do { u32 tgt_ = 6000; if (PROBE_ST == 99) { while ((u32)__builtin_amdgcn_s_memrealtime() - stamps[0] < 5000) __builtin_amdgcn_s_sleep(1); \
    tgt_ += ((u32)__builtin_amdgcn_s_memtime() - cyc0) / 10; } \
  else if (PROBE_ST >= 0 && (PROBE_COND) && stamps[PROBE_ST < 0 || PROBE_ST > 11 ? 0 : PROBE_ST]) { const u32 d_ = stamps[PROBE_ST < 0 || PROBE_ST > 11 ? 0 : PROBE_ST] - stamps[0]; tgt_ += 4 * (PROBE_SIGN > 0 ? d_ : 4000 - d_); } \
  if (bid == 0) while ((u32)__builtin_amdgcn_s_memrealtime() - stamps[0] < tgt_) __builtin_amdgcn_s_sleep(1); } while (0)

__global__ void __launch_bounds__(512, 2) fused_decode(Params p) {
  __shared__ __attribute__((aligned(1024))) char lds_raw[L_END];
  LAS char* lds = (LAS char*)lds_raw;
  const u32 lbase = (u32)(uintptr_t)lds;
  const int tid = threadIdx.x, lane = tid & 63, wave = __builtin_amdgcn_readfirstlane(tid >> 6);
  const int bid = blockIdx.x, xc = bid & 7, jc = bid >> 3;
  const int hk = 2 * xc + (jc & 1), half = (jc >> 1) & 1, bat = jc >> 2;
  const int g = lane >> 4, n = lane & 15;
  STAMP_DECL;
  STAMP(0);
  u32 ep;
  { const u32 z = 0; asm volatile("global_load_dword %0, %1, %2 sc1" : "=v"(ep) : "v"(z), "s"(p.ws) : "memory"); }

  if (wave < 4) {
    const size_t reg = ((size_t)(bat * 16 + hk) * 4096 + 3072 + half * 512 + wave * 128) * 256;
    const char* kb = p.k + reg;
    const char* vb = p.v + reg;
    const u32 rot = (((u32)(bid * 4 + wave) * 2654435761u) >> 20) & 7;
    const u32 koff = n * 256 + g * 16, voff = g * 1024 + n * 16;
    v4u K[32], V[32];
#define LDK(t) do { const char* b_ = kb + (((t) + rot) & 7) * 4096; \
    asm volatile("global_load_dwordx4 %0, %4, %5" KV_POL "\n\tglobal_load_dwordx4 %1, %4, %5 offset:64" KV_POL "\n\t" \
                 "global_load_dwordx4 %2, %4, %5 offset:128" KV_POL "\n\tglobal_load_dwordx4 %3, %4, %5 offset:192" KV_POL \
                 : "=&v"(K[4 * (t)]), "=&v"(K[4 * (t) + 1]), "=&v"(K[4 * (t) + 2]), "=&v"(K[4 * (t) + 3]) : "v"(koff), "s"(b_) : "memory"); } while (0)
#define LDV(t) do { const char* b_ = vb + (((t) + rot) & 7) * 4096; \
    asm volatile("global_load_dwordx4 %0, %4, %5" V_POL "\n\tglobal_load_dwordx4 %1, %4, %5 offset:256" V_POL "\n\t" \
                 "global_load_dwordx4 %2, %4, %5 offset:512" V_POL "\n\tglobal_load_dwordx4 %3, %4, %5 offset:768" V_POL \
                 : "=&v"(V[4 * (t)]), "=&v"(V[4 * (t) + 1]), "=&v"(V[4 * (t) + 2]), "=&v"(V[4 * (t) + 3]) : "v"(voff), "s"(b_) : "memory"); } while (0)
#define TIE4(A, t) asm volatile("" : "+v"(A[4 * (t)]), "+v"(A[4 * (t) + 1]), "+v"(A[4 * (t) + 2]), "+v"(A[4 * (t) + 3]))
    LDK(0); LDK(1); LDK(2);
    u32 T;
    asm volatile("s_waitcnt vmcnt(12)\n\tv_readfirstlane_b32 %0, %1\n\ts_nop 1" : "=s"(T) : "v"(ep) : "memory");
    T += 1u;
    __builtin_amdgcn_s_barrier();
    asm volatile("" ::: "memory");
    STAMP(1);
    LDK(3); LDK(4); LDK(5); LDK(6); LDK(7);
    LDV(0); LDV(1); LDV(2); LDV(3);
    STAMP(2);
    lds_wait_ge(lbase + C_Q, 1);
    STAMP(3);
    v4u qf[4];
#pragma unroll
    for (int ks = 0; ks < 4; ++ks) qf[ks] = *(LAS v4u*)(lds + L_Q + ((n >> 3) & 1) * 256 + ks * 64 + g * 16);
    v4f s[8];
    VMCNT(32);
    TIE4(K, 0); TIE4(K, 1); TIE4(K, 2); TIE4(K, 3);
#pragma unroll
    for (int t = 0; t < 4; ++t) {
      s[t] = v4f{0.f, 0.f, 0.f, 0.f};
#pragma unroll
      for (int ks = 0; ks < 4; ++ks) s[t] = mfma(K[4 * t + ks], qf[ks], s[t]);
    }
    asm volatile("s_nop 7\n\ts_nop 1" : "+v"(s[0]), "+v"(s[1]), "+v"(s[2]), "+v"(s[3]));
    LDV(4); LDV(5); LDV(6); LDV(7);
    lds_inc(lbase + C_VI);
    STAMP(4);
    VMCNT(32);
    TIE4(K, 4); TIE4(K, 5); TIE4(K, 6); TIE4(K, 7);
#pragma unroll
    for (int t = 4; t < 8; ++t) {
      s[t] = v4f{0.f, 0.f, 0.f, 0.f};
#pragma unroll
      for (int ks = 0; ks < 4; ++ks) s[t] = mfma(K[4 * t + ks], qf[ks], s[t]);
    }
    constexpr float C2 = 0.08838834764831845f * 1.4426950408889634f;
    float mx = s[0][0];
#pragma unroll
    for (int t = 0; t < 8; ++t)
#pragma unroll
      for (int r = 0; r < 4; ++r) mx = fmaxf(mx, s[t][r]);
    mx = xg_max(mx) * C2;
    float ls = 0.f;
#pragma unroll
    for (int t = 0; t < 8; ++t)
#pragma unroll
      for (int r = 0; r < 4; ++r) { s[t][r] = __builtin_amdgcn_exp2f(fmaf(s[t][r], C2, -mx)); ls += s[t][r]; }
    ls = xg_sum(ls);
    if ((lane & 0x37) == 0) *(LAS v2f*)(lds + L_ML + wave * 16 + (lane >> 3) * 8) = v2f{mx, ls};
    lds_inc(lbase + C_ML);
    u32 msk[4];
#pragma unroll
    for (int i = 0; i < 4; ++i) msk[i] = (((lane & 7) >> 1) == i) ? ((lane & 1) ? 0xffff0000u : 0x0000ffffu) : 0u;
    v4f acc = {0.f, 0.f, 0.f, 0.f};
    VMCNT(16);
    TIE4(V, 0); TIE4(V, 1); TIE4(V, 2); TIE4(V, 3);
#pragma unroll
    for (int i = 0; i < 16; ++i) {
      const u32 pp = pk_bf16(s[i >> 2][i & 3], s[i >> 2][i & 3]);
      acc = mfma(V[i], v4u{pp & msk[0], pp & msk[1], pp & msk[2], pp & msk[3]}, acc);
    }
    v4u pb[16];
#pragma unroll
    for (int i = 0; i < 16; ++i) {
      const u32 pp = pk_bf16(s[4 + (i >> 2)][i & 3], s[4 + (i >> 2)][i & 3]);
      pb[i] = v4u{pp & msk[0], pp & msk[1], pp & msk[2], pp & msk[3]};
    }
    lds_wait_ge(lbase + C_ML, 4);
    float fw;
    {
      v2f ml[4];
#pragma unroll
      for (int w = 0; w < 4; ++w) ml[w] = *(LAS v2f*)(lds + L_ML + w * 16 + ((n >> 3) & 1) * 8);
      const float M = fmaxf(fmaxf(ml[0].x, ml[1].x), fmaxf(ml[2].x, ml[3].x));
      float L = 0.f;
#pragma unroll
      for (int w = 0; w < 4; ++w) L = fmaf(ml[w].y, __builtin_amdgcn_exp2f(ml[w].x - M), L);
      fw = __builtin_amdgcn_exp2f(mx - M) / L;
    }
    TIE4(pb, 0); TIE4(pb, 1); TIE4(pb, 2); TIE4(pb, 3);
    asm volatile("" : "+v"(fw), "+v"(acc));
    STAMP(5);
    VMCNT(12); TIE4(V, 4);
#pragma unroll
    for (int i = 0; i < 4; ++i) acc = mfma(V[16 + i], pb[i], acc);
    asm volatile("" : "+v"(acc));
    VMCNT(8); TIE4(V, 5);
#pragma unroll
    for (int i = 4; i < 8; ++i) acc = mfma(V[16 + i], pb[i], acc);
    asm volatile("" : "+v"(acc));
    VMCNT(4); TIE4(V, 6);
#pragma unroll
    for (int i = 8; i < 12; ++i) acc = mfma(V[16 + i], pb[i], acc);
    asm volatile("" : "+v"(acc));
    VMCNT(2); asm volatile("" : "+v"(V[28]), "+v"(V[29]));
    acc = mfma(V[28], pb[12], acc); acc = mfma(V[29], pb[13], acc);
    asm volatile("" : "+v"(acc));
    VMCNT(0); asm volatile("" : "+v"(V[30]), "+v"(V[31]));
    acc = mfma(V[30], pb[14], acc); acc = mfma(V[31], pb[15], acc);
    STAMP(6);
    acc *= fw;
    {
      const u32 a_own = lbase + L_OS + wave * 1024 + lane * 16, a_all = lbase + L_OS + lane * 16, a_cnt = lbase + C_O, one = 1;
      u64 keep; u32 tk; v4f o0, o1, o2, o3;
      asm volatile("ds_write_b128 %6, %7\n\t"
                   "s_mov_b64 %0, exec\n\ts_mov_b64 exec, 1\n\tds_add_rtn_u32 %1, %8, %9\n\ts_mov_b64 exec, %0\n\t"
                   "ds_read_b128 %2, %10\n\tds_read_b128 %3, %10 offset:1024\n\tds_read_b128 %4, %10 offset:2048\n\tds_read_b128 %5, %10 offset:3072\n\t"
                   "s_waitcnt lgkmcnt(0)"
                   : "=&s"(keep), "=&v"(tk), "=&v"(o0), "=&v"(o1), "=&v"(o2), "=&v"(o3)
                   : "v"(a_own), "v"(acc), "v"(a_cnt), "v"(one), "v"(a_all) : "memory");
      if (__builtin_amdgcn_readfirstlane(tk) == 3) {
        const v4f o = (o0 + o1) + (o2 + o3);
        const u32 off = WS_O + (2 * hk + ((n >> 3) & 1)) * 8192 + (2 * bat + half) * 512 + (8 * g + (n & 7)) * 16;
        st16_dev(p.ws, off, v4u{pk_bf16(o[0], o[1]), T, pk_bf16(o[2], o[3]), T});
      }
    }
    STAMP(7);
    if (wave == 0 || (wave == 1 && bid < 32)) {
      const int tau = wave == 0 ? bid : 256 + bid;
      const u32 po = WS_P + (lane >> 3) * P_ROW + tau * 128 + (lane & 7) * 16;
      const u32 go = WS_GATE + (lane & 3) * 16;
      const u32 po1 = po + 8 * P_ROW, po2 = po + 16 * P_ROW, po3 = po + 24 * P_ROW, po4 = po + 32 * P_ROW, po5 = po + 40 * P_ROW;
      bool first = true;
      _Pragma("nounroll") for (u32 spins = 0; spins < SPIN_CAP; ++spins) {
        v4u a0, a1, a2, a3, a4, a5, gg;
        asm volatile("global_load_dwordx4 %0, %7, %14 sc1\n\tglobal_load_dwordx4 %1, %8, %14 sc1\n\tglobal_load_dwordx4 %2, %9, %14 sc1\n\t"
                     "global_load_dwordx4 %3, %10, %14 sc1\n\tglobal_load_dwordx4 %4, %11, %14 sc1\n\tglobal_load_dwordx4 %5, %12, %14 sc1\n\t"
                     "global_load_dwordx4 %6, %13, %14 sc1\n\ts_waitcnt vmcnt(0)"
                     : "=&v"(a0), "=&v"(a1), "=&v"(a2), "=&v"(a3), "=&v"(a4), "=&v"(a5), "=&v"(gg)
                     : "v"(po), "v"(po1), "v"(po2), "v"(po3), "v"(po4), "v"(po5), "v"(go), "s"(p.ws) : "memory");
        const u32 bad = (a0.y ^ T) | (a0.w ^ T) | (a1.y ^ T) | (a1.w ^ T) | (a2.y ^ T) | (a2.w ^ T) | (a3.y ^ T) | (a3.w ^ T) |
                        (a4.y ^ T) | (a4.w ^ T) | (a5.y ^ T) | (a5.w ^ T) | (gg.y ^ T) | (gg.w ^ T);
        const bool ok = __builtin_amdgcn_ballot_w64(bad != 0) == 0;
        if (ok || first) {
          first = false;
#define F_(v) __uint_as_float(v)
          const float g0 = F_(gg.x), g1 = F_(gg.z);
          float v0 = g0 * ((F_(a0.x) + F_(a1.x)) + (F_(a2.x) + F_(a3.x))) + (1.f - g0) * (F_(a4.x) + F_(a5.x));
          float v1 = g1 * ((F_(a0.z) + F_(a1.z)) + (F_(a2.z) + F_(a3.z))) + (1.f - g1) * (F_(a4.z) + F_(a5.z));
          v0 += dpp<ROR8>(v0); v1 += dpp<ROR8>(v1);
          v0 = xg_sum(v0); v1 = xg_sum(v1);
          const float w0 = dpp<ROR4>(v0), w1 = dpp<ROR4>(v1);
          const bool hi = (lane & 4) != 0;
          const u32 r = hi ? pk_bf16(w1, v1) : pk_bf16(v0, w0);
          if (ok) {
            if (lane < 8) *(u32*)(p.out + ((2 * (lane & 3) + (hi ? 1 : 0)) * 576 + 2 * tau) * 2) = r;
            if (tau == 0 && lane == 0) __hip_atomic_store((u32*)(p.ws + WS_EPOCH), T, __ATOMIC_RELAXED, __HIP_MEMORY_SCOPE_AGENT);
            break;
          }
          asm volatile("" :: "v"(r));
        }
        __builtin_amdgcn_s_sleep(1);
      }
      STAMP(8);
    }
    STAMP_FLUSH();
    return;
  }

  const int hw = wave - 4;
  __builtin_amdgcn_s_setprio(3);
  const int drow = lane >> 3, dch = (lane & 7) ^ ((drow >> 1) & 3);
  const bool isS = jc >= 26, isQ = jc >= 18 && !isS;
  const int unit = isS ? xc * 6 + (jc - 26) : isQ ? xc * 8 + (jc - 18) : xc * 18 + jc;
  const char* t1src = nullptr; size_t t1step = 0;
  if (!isS) {
#pragma unroll
    for (int i = 0; i < 3; ++i) { const int c = min(hw + 4 * i, 8); DMA16(p.x + c * 1024 + lane * 16, lbase + L_X + c * 1024, ""); }
    if (isQ) { t1src = p.wq + (size_t)(144 * hw + drow) * 8192 + unit * 128 + dch * 16; t1step = 8 * 8192; }
    else { t1src = (dch < 4 ? p.wg : p.wu) + (size_t)(144 * hw + drow) * 9216 + unit * 64 + (dch & 3) * 16; t1step = 8 * 9216; }
#pragma unroll
    for (int i = 0; i < 8; ++i) { DMA16(t1src, lbase + L_T1 + (18 * hw + i) * 1024, W_POL); t1src += t1step; }
    VMCNT(11);
  } else {
    VMCNT(0);
  }
  u32 T;
  asm volatile("v_readfirstlane_b32 %0, %1\n\ts_nop 1" : "=s"(T) : "v"(ep) : "memory");
  T += 1u;
  if (hw == 0 && lane < 16) *(LAS u32*)(lds + L_CTL + lane * 4) = 0u;
  asm volatile("s_waitcnt lgkmcnt(0)" ::: "memory");
  __builtin_amdgcn_s_barrier();
  asm volatile("" ::: "memory");
  STAMP(1);
  const int beta = 2 * (n >> 2) + (n & 1);
  if (!isS) {
#pragma unroll
    for (int i = 8; i < 18; ++i) { DMA16(t1src, lbase + L_T1 + (18 * hw + i) * 1024, W_POL); t1src += t1step; }
    VMCNT(0);
    lds_inc(lbase + C_S1);
    lds_wait_ge(lbase + C_S1, 4);
    STAMP(2);
    const u32 ta = L_T1 + tr_lane_addr(lane, hw);
    const u32 xa = L_X + beta * 1152 + g * 8;
    v4f acc = {0.f, 0.f, 0.f, 0.f};
#pragma unroll
    for (int ks = 0; ks < 18; ++ks) {
      const v2u x0 = *(LAS v2u*)(lds + xa + ks * 64), x1 = *(LAS v2u*)(lds + xa + ks * 64 + 32);
      acc = mfma(v4u{x0.x, x0.y, x1.x, x1.y}, trfrag(lds, ta, ks * 4096), acc);
    }
    if (isQ) {
      const float n0 = dpp<XOR1>(acc[0]), n1 = dpp<XOR1>(acc[1]);
      const u32 pk = (n & 1) ? pk_bf16(n1, acc[1]) : pk_bf16(acc[0], n0);
      const int col = 64 * unit + (n >> 3) * 32 + 8 * hw + (n & 7);
      st8_dev(p.ws, WS_Q + ((2 * g + (n & 1)) * 2048 + (col >> 1)) * 8, v2u{pk, T});
    } else {
      const float o0 = dpp<ROR8>(acc[0]), o1 = dpp<ROR8>(acc[1]);
      const float gv = bf16r(n < 8 ? acc[0] : o1), uv = bf16r(n < 8 ? o0 : acc[1]);
      const float sg = bf16r(gv / (1.f + __expf(-gv)));
      const float hv = sg * uv, hn = dpp<XOR1>(hv);
      const int col = 32 * unit + 8 * hw + (n & 7);
      if ((n & 1) == 0) st8_dev(p.ws, WS_H + ((2 * g + (n >> 3)) * 2304 + (col >> 1)) * 8, v2u{pk_bf16(hv, hn), T});
    }
    STAMP(3);
  }

  if (hw == 3) {
    if (isS && unit == 0) {
      const v4u* xp = (const v4u*)(p.x + (lane >> 3) * 1152) + (lane & 7);
      const v4u* gp = (const v4u*)p.gw + (lane & 7);
      float sum = 0.f;
#pragma unroll
      for (int i = 0; i < 9; ++i) {
        const v4u a = xp[8 * i], b = gp[8 * i];
#pragma unroll
        for (int e = 0; e < 4; ++e) {
          sum = fmaf(__uint_as_float(a[e] << 16), __uint_as_float(b[e] << 16), sum);
          sum = fmaf(__uint_as_float(a[e] & 0xffff0000u), __uint_as_float(b[e] & 0xffff0000u), sum);
        }
      }
      sum += dpp<XOR1>(sum); sum += dpp<XOR2>(sum); sum += dpp<HMIR>(sum);
      const float gt = 1.f / (1.f + __expf(-sum));
      if ((lane & 7) == 0) st8_dev(p.ws, WS_GATE + (lane >> 3) * 8, v2u{__float_as_uint(gt), T});
    }
    const u32 qo = WS_Q + (bat * 2048 + hk * 128) * 8 + lane * 16;
    v4u qv;
    _Pragma("nounroll") for (u32 spins = 0; spins < SPIN_CAP; ++spins) {
      asm volatile("global_load_dwordx4 %0, %1, %2 sc1\n\ts_waitcnt vmcnt(0)" : "=&v"(qv) : "v"(qo), "s"(p.ws) : "memory");
      if (__builtin_amdgcn_ballot_w64(((qv.y ^ T) | (qv.w ^ T)) != 0) == 0) break;
      __builtin_amdgcn_s_sleep(4);
    }
    *(LAS v2u*)(lds + L_Q + lane * 8) = v2u{qv.x, qv.z};
    lds_inc(lbase + C_Q);
    STAMP(4);
    lds_wait_ge(lbase + C_ML, 4);
    if (lane < 2) {
      v2f ml[4];
#pragma unroll
      for (int w = 0; w < 4; ++w) ml[w] = *(LAS v2f*)(lds + L_ML + w * 16 + lane * 8);
      const float M = fmaxf(fmaxf(ml[0].x, ml[1].x), fmaxf(ml[2].x, ml[3].x));
      float L = 0.f;
#pragma unroll
      for (int w = 0; w < 4; ++w) L = fmaf(ml[w].y, __builtin_amdgcn_exp2f(ml[w].x - M), L);
      st8_dev(p.ws, WS_LSE + ((2 * hk + lane) * 16 + 2 * bat + half) * 8, v2u{__float_as_uint(M + __builtin_amdgcn_logf(L)), T});
    }
    STAMP(5);
    STAMP_FLUSH();
    return;
  }

  const u32 pst = ((n & 7) + (n >> 3) * 32) * 64 + g * 16;
  if (isS) {
    const int rg = unit / 3, cg = 3 * (unit % 3) + hw;
    v4u A[9];
    {
      const u32 ho = WS_H + (beta * 2304 + rg * 144) * 8 + g * 16;
      _Pragma("nounroll") for (u32 spins = 0; spins < SPIN_CAP; ++spins) {
        v4u h[18];
#define LH(i) "global_load_dwordx4 %" #i ", %18, %19 offset:" #i "*64 sc1\n\t"
        asm volatile(LH(0) LH(1) LH(2) LH(3) LH(4) LH(5) LH(6) LH(7) LH(8) LH(9) LH(10) LH(11) LH(12) LH(13) LH(14) LH(15) LH(16) LH(17) "s_waitcnt vmcnt(0)"
                     : "=&v"(h[0]), "=&v"(h[1]), "=&v"(h[2]), "=&v"(h[3]), "=&v"(h[4]), "=&v"(h[5]), "=&v"(h[6]), "=&v"(h[7]), "=&v"(h[8]),
                       "=&v"(h[9]), "=&v"(h[10]), "=&v"(h[11]), "=&v"(h[12]), "=&v"(h[13]), "=&v"(h[14]), "=&v"(h[15]), "=&v"(h[16]), "=&v"(h[17])
                     : "v"(ho), "s"(p.ws) : "memory");
        u32 bad = 0;
#pragma unroll
        for (int i = 0; i < 18; ++i) bad |= (h[i].y ^ T) | (h[i].w ^ T);
#pragma unroll
        for (int ks = 0; ks < 9; ++ks) A[ks] = v4u{h[2 * ks].x, h[2 * ks].z, h[2 * ks + 1].x, h[2 * ks + 1].z};
        if (__builtin_amdgcn_ballot_w64(bad != 0) == 0) break;
        __builtin_amdgcn_s_sleep(32);
      }
    }
    STAMP(4);
    lds_wait_ge(lbase + C_VI, 4);
    STAMP(5);
    const char* src = p.wd + (size_t)(288 * rg + drow) * 1152 + cg * 128 + dch * 16;
    const u32 tb = L_T1 + hw * 36864;
#pragma unroll
    for (int i = 0; i < 36; ++i) { DMA16(src, lbase + tb + i * 1024, W_POL); src += 8 * 1152; }
    STAMP(6);
    u32 ta[4];
    v4f acc[4];
#pragma unroll
    for (int j = 0; j < 4; ++j) { ta[j] = tb + tr_lane_addr(lane, j); acc[j] = v4f{0.f, 0.f, 0.f, 0.f}; }
#define DSTEP(ks, N) do { VMCNT(N); _Pragma("unroll") for (int j = 0; j < 4; ++j) acc[j] = mfma(A[ks], trfrag(lds, ta[j], (ks) * 4096), acc[j]); } while (0)
    DSTEP(0, 32); DSTEP(1, 28); DSTEP(2, 24); DSTEP(3, 20); DSTEP(4, 16); DSTEP(5, 12); DSTEP(6, 8); DSTEP(7, 4); DSTEP(8, 0);
    const u32 po = WS_P + (32 + rg) * P_ROW + cg * 4096 + pst;
#pragma unroll
    for (int j = 0; j < 4; ++j) st16_dev(p.ws, po + j * 512, v4u{__float_as_uint(acc[j][0]), T, __float_as_uint(acc[j][1]), T});
    STAMP(8);
    STAMP_FLUSH();
    return;
  }
  if (hw == 0 || (hw == 1 && jc < 10)) {
    const int task = hw == 0 ? jc : 26 + jc, head = 4 * xc + task / 9, cg = task % 9;
    lds_wait_ge(lbase + C_VI, 4);
    STAMP(4);
    const char* src = p.wo + (size_t)(128 * head + 8 * (drow & 3) + (drow >> 2)) * 1152 + cg * 128 + dch * 16;
    const u32 tb = L_WO + hw * 16384;
#pragma unroll
    for (int i = 0; i < 16; ++i) { const char* s_ = src + (32 * (i >> 2) + 2 * (i & 3)) * 1152; DMA16(s_, lbase + tb + i * 1024, W_POL); }
    float w0, w1, w2, w3;
    {
      const u32 lo = WS_LSE + (head * 16 + 4 * g) * 8;
      v4u e0, e1;
      _Pragma("nounroll") for (u32 spins = 0; spins < SPIN_CAP; ++spins) {
        asm volatile("global_load_dwordx4 %0, %2, %3 sc1\n\tglobal_load_dwordx4 %1, %2, %3 offset:16 sc1\n\ts_waitcnt vmcnt(0)"
                     : "=&v"(e0), "=&v"(e1) : "v"(lo), "s"(p.ws) : "memory");
        if (__builtin_amdgcn_ballot_w64(((e0.y ^ T) | (e0.w ^ T) | (e1.y ^ T) | (e1.w ^ T)) != 0) == 0) break;
        __builtin_amdgcn_s_sleep(2);
      }
      const float d0 = __uint_as_float(e0.z) - __uint_as_float(e0.x), d1 = __uint_as_float(e1.z) - __uint_as_float(e1.x);
      w0 = 1.f / (1.f + __builtin_amdgcn_exp2f(d0)); w1 = 1.f / (1.f + __builtin_amdgcn_exp2f(-d0));
      w2 = 1.f / (1.f + __builtin_amdgcn_exp2f(d1)); w3 = 1.f / (1.f + __builtin_amdgcn_exp2f(-d1));
    }
    STAMP(5);
    v4u B[16];
#pragma unroll
    for (int j = 0; j < 4; ++j) {
      const u32 ta = tb + tr_lane_addr(lane, j);
#pragma unroll
      for (int ks = 0; ks < 4; ++ks) B[4 * j + ks] = trfrag(lds, ta, ks * 4096);
    }
    TIE4(B, 0); TIE4(B, 1); TIE4(B, 2); TIE4(B, 3);
    STAMP(6);
    const u32 oo = WS_O + head * 8192 + n * 512 + g * 16;
    const u32 po = WS_P + head * P_ROW + cg * 4096 + pst;
    bool first = true;
    _Pragma("nounroll") for (u32 spins = 0; spins < SPIN_CAP; ++spins) {
      v4u u[8];
#define LO(i) "global_load_dwordx4 %" #i ", %8, %9 offset:" #i "*64 sc1\n\t"
      asm volatile(LO(0) LO(1) LO(2) LO(3) LO(4) LO(5) LO(6) LO(7) "s_waitcnt vmcnt(0)"
                   : "=&v"(u[0]), "=&v"(u[1]), "=&v"(u[2]), "=&v"(u[3]), "=&v"(u[4]), "=&v"(u[5]), "=&v"(u[6]), "=&v"(u[7])
                   : "v"(oo), "s"(p.ws) : "memory");
      u32 bad = 0;
#pragma unroll
      for (int i = 0; i < 8; ++i) bad |= (u[i].y ^ T) | (u[i].w ^ T);
      const bool ok = __builtin_amdgcn_ballot_w64(bad != 0) == 0;
      if (ok || first) {
        first = false;
        v4f acc[4];
#pragma unroll
        for (int j = 0; j < 4; ++j) acc[j] = v4f{0.f, 0.f, 0.f, 0.f};
#pragma unroll
        for (int ks = 0; ks < 4; ++ks) {
          const v4u A = {u[2 * ks].x, u[2 * ks].z, u[2 * ks + 1].x, u[2 * ks + 1].z};
#pragma unroll
          for (int j = 0; j < 4; ++j) acc[j] = mfma(A, B[4 * j + ks], acc[j]);
        }
        v4u r[4];
#pragma unroll
        for (int j = 0; j < 4; ++j)
          r[j] = v4u{__float_as_uint(fmaf(w0, acc[j][0], w1 * acc[j][1])), T, __float_as_uint(fmaf(w2, acc[j][2], w3 * acc[j][3])), T};
        if (ok) {
#pragma unroll
          for (int j = 0; j < 4; ++j) st16_dev(p.ws, po + j * 512, r[j]);
          break;
        }
        asm volatile("" :: "v"(r[0]), "v"(r[1]), "v"(r[2]), "v"(r[3]));
      }
      __builtin_amdgcn_s_sleep(1);
    }
    STAMP(8);
  }
  STAMP_FLUSH();
}

extern "C" void kernel_launch(void* const* d_in, const int* in_sizes, int n_in, void* d_out, int out_size, void* d_ws, size_t ws_size,
                              hipStream_t stream) {
  Params p;
  p.x = (const char*)d_in[0]; p.k = (const char*)d_in[1]; p.v = (const char*)d_in[2]; p.wq = (const char*)d_in[3]; p.wo = (const char*)d_in[4];
  p.wg = (const char*)d_in[6]; p.wu = (const char*)d_in[7]; p.wd = (const char*)d_in[8]; p.gw = (const char*)d_in[9];
  p.out = (char*)d_out; p.ws = (char*)d_ws;
  hipLaunchKernelGGL(fused_decode, dim3(256), dim3(512), 0, stream, p);
}
```

```cpp
#define PRESTREAM
#define PK_POL " nt"
#define PV_POL " nt"
#define PW_POL ""
#define PROBE_ST 11
#define PROBE_COND 1
#define PROBE_SIGN 1
#include <hip/hip_runtime.h>
#include <stdint.h>

typedef unsigned u32;
typedef unsigned long long u64;
typedef u32 v4u __attribute__((ext_vector_type(4)));
typedef u32 v2u __attribute__((ext_vector_type(2)));
typedef float v4f __attribute__((ext_vector_type(4)));
typedef float v2f __attribute__((ext_vector_type(2)));
typedef short v4s __attribute__((ext_vector_type(4)));
typedef __bf16 v8bf __attribute__((ext_vector_type(8)));
typedef __bf16 v2bf __attribute__((ext_vector_type(2)));
#define LAS __attribute__((address_space(3)))
#define DEVINL __device__ __forceinline__

#define KV_POL " nt"
#define V_POL KV_POL
#define W_POL " nt"

constexpr u32 WS_EPOCH = 0;
constexpr u32 WS_GATE = 256;
constexpr u32 WS_LSE = 4096;
constexpr u32 WS_Q = 16384;
constexpr u32 WS_H = WS_Q + 8 * 2048 * 8;
constexpr u32 WS_O = WS_H + 8 * 2304 * 8;
constexpr u32 WS_P = WS_O + 32 * 16 * 512;
constexpr u32 P_ROW = 576 * 64;
constexpr u32 WS_DIAG = 4u << 20;
constexpr u32 L_T1 = 0;
constexpr u32 L_X = 73728;
constexpr u32 L_WO = L_X + 9216;
constexpr u32 L_Q = L_WO + 2 * 16384;
constexpr u32 L_ML = L_Q + 512;
constexpr u32 L_CTL = L_ML + 64;
constexpr u32 L_OS = L_CTL + 64;
constexpr u32 L_END = L_OS + 4096;
constexpr u32 C_S1 = L_CTL + 0, C_Q = L_CTL + 4, C_ML = L_CTL + 8, C_O = L_CTL + 12, C_VI = L_CTL + 16;
constexpr u32 SPIN_CAP = 1u << 17;

struct Params {
  const char* x; const char* k; const char* v; const char* wq; const char* wo; const char* wg; const char* wu; const char* wd;
  const char* gw; char* out; char* ws;
};

DEVINL v4f mfma(v4u a, v4u b, v4f c) {
  return __builtin_amdgcn_mfma_f32_16x16x32_bf16(__builtin_bit_cast(v8bf, a), __builtin_bit_cast(v8bf, b), c, 0, 0, 0);
}
DEVINL u32 pk_bf16(float lo, float hi) { v2f f = {lo, hi}; return __builtin_bit_cast(u32, __builtin_convertvector(f, v2bf)); }
DEVINL float bf16r(float f) { return __builtin_bit_cast(float, pk_bf16(f, f) & 0xffff0000u); }
template <int CTRL> DEVINL float dpp(float x) {
  return __builtin_bit_cast(float, __builtin_amdgcn_mov_dpp(__builtin_bit_cast(int, x), CTRL, 0xf, 0xf, true));
}
constexpr int XOR1 = 0xB1, XOR2 = 0x4E, ROR4 = 0x124, ROR8 = 0x128, HMIR = 0x141;
DEVINL float xg_max(float x) {
  auto s = __builtin_amdgcn_permlane16_swap(__float_as_uint(x), __float_as_uint(x), false, false);
  x = fmaxf(__uint_as_float(s[0]), __uint_as_float(s[1]));
  auto t = __builtin_amdgcn_permlane32_swap(__float_as_uint(x), __float_as_uint(x), false, false);
  return fmaxf(__uint_as_float(t[0]), __uint_as_float(t[1]));
}
DEVINL float xg_sum(float x) {
  auto s = __builtin_amdgcn_permlane16_swap(__float_as_uint(x), __float_as_uint(x), false, false);
  x = __uint_as_float(s[0]) + __uint_as_float(s[1]);
  auto t = __builtin_amdgcn_permlane32_swap(__float_as_uint(x), __float_as_uint(x), false, false);
  return __uint_as_float(t[0]) + __uint_as_float(t[1]);
}
DEVINL void lds_inc(u32 addr) {
  u64 keep; const u32 one = 1;
  asm volatile("s_mov_b64 %0, exec\n\ts_mov_b64 exec, 1\n\tds_add_u32 %1, %2\n\ts_mov_b64 exec, %0" : "=&s"(keep) : "v"(addr), "v"(one) : "memory");
}
DEVINL void lds_wait_ge(u32 addr, u32 want) {
  _Pragma("nounroll") for (u32 spins = 0; spins < (SPIN_CAP << 4); ++spins) {
    u32 r;
    asm volatile("ds_read_b32 %0, %1\n\ts_waitcnt lgkmcnt(0)" : "=v"(r) : "v"(addr) : "memory");
    if ((u32)__builtin_amdgcn_readfirstlane(r) >= want) break;
    __builtin_amdgcn_s_sleep(1);
  }
}
#define DMA16(gptr, ldsdst, POL) do { u32 keep_; \
  asm volatile("s_mov_b32 %0, m0\n\ts_mov_b32 m0, %2\n\ts_nop 0\n\tglobal_load_lds_dwordx4 %1, off" POL "\n\ts_mov_b32 m0, %0" \
               : "=&s"(keep_) : "v"(gptr), "s"(ldsdst) : "memory"); } while (0)
#define VMCNT(n) asm volatile("s_waitcnt vmcnt(" #n ")" ::: "memory")
DEVINL void st16_dev(char* base, u32 off, v4u d) { asm volatile("global_store_dwordx4 %0, %1, %2 sc1\n\ts_nop 1" :: "v"(off), "v"(d), "s"(base) : "memory"); }
DEVINL void st8_dev(char* base, u32 off, v2u d) { asm volatile("global_store_dwordx2 %0, %1, %2 sc1" :: "v"(off), "v"(d), "s"(base) : "memory"); }
DEVINL v4u trfrag(LAS char* l, u32 a0, u32 imm) {
  const v2u x = __builtin_bit_cast(v2u, __builtin_amdgcn_ds_read_tr16_b64_v4i16((LAS v4s*)(l + a0 + imm)));
  const v2u y = __builtin_bit_cast(v2u, __builtin_amdgcn_ds_read_tr16_b64_v4i16((LAS v4s*)(l + a0 + imm + 2048)));
  return v4u{x.x, x.y, y.x, y.y};
}
DEVINL u32 tr_lane_addr(int lane, int j) {
  const int r0 = (lane >> 2) & 15, p = lane & 3;
  const int c = (p < 2 ? j : j + 4) ^ ((r0 >> 1) & 3);
  return r0 * 128 + c * 16 + 8 * (p & 1);
}

#define STAMP(i) do { stamps[i] = (u32)__builtin_amdgcn_s_memrealtime(); } while (0)
#define STAMP_DECL u32 stamps[12] = {0, 0, 0, 0, 0, 0, 0, 0, 0, 0, 0, 0}; const u32 cyc0 = (u32)__builtin_amdgcn_s_memtime()
#define STAMP_FLUSH() do { u32 tgt_ = 8000; if (PROBE_ST == 99) { while ((u32)__builtin_amdgcn_s_memrealtime() - stamps[0] < 5000) __builtin_amdgcn_s_sleep(1); \
    tgt_ += ((u32)__builtin_amdgcn_s_memtime() - cyc0) / 10; } \
  else if (PROBE_ST >= 0 && (PROBE_COND) && stamps[PROBE_ST < 0 || PROBE_ST > 11 ? 0 : PROBE_ST]) { const u32 d_ = stamps[PROBE_ST < 0 || PROBE_ST > 11 ? 0 : PROBE_ST] - stamps[0]; tgt_ += 4 * (PROBE_SIGN > 0 ? d_ : 4000 - d_); } \
  while ((u32)__builtin_amdgcn_s_memrealtime() - stamps[0] < tgt_) __builtin_amdgcn_s_sleep(1); } while (0)

__global__ void __launch_bounds__(512, 2) fused_decode(Params p) {
  __shared__ __attribute__((aligned(1024))) char lds_raw[L_END];
  LAS char* lds = (LAS char*)lds_raw;
  const u32 lbase = (u32)(uintptr_t)lds;
  const int tid = threadIdx.x, lane = tid & 63, wave = __builtin_amdgcn_readfirstlane(tid >> 6);
  const int bid = blockIdx.x, xc = bid & 7, jc = bid >> 3;
  const int hk = 2 * xc + (jc & 1), half = (jc >> 1) & 1, bat = jc >> 2;
  const int g = lane >> 4, n = lane & 15;
  STAMP_DECL;
  STAMP(0);

  {
    const int half_ = (bid >> 2) & 1, pair_ = (bid & 3) | ((bid >> 3) << 2);
    const size_t base_ = ((size_t)pair_ * 4096 + 3072 + half_ * 512 + wave * 64) * 256;
    const int rot_ = (((bid * 8 + wave) * 2654435761u) >> 20) % 16;
    v4u kr[16], vr[16], wr[12];
    const u32 lo_ = lane * 16;
#pragma unroll
    for (int i = 0; i < 16; ++i) { const char* b_ = p.k + base_ + ((i + rot_) & 15) * 1024; asm volatile("global_load_dwordx4 %0, %1, %2" PK_POL : "=v"(kr[i]) : "v"(lo_), "s"(b_) : "memory"); }
#pragma unroll
    for (int i = 0; i < 16; ++i) { const char* b_ = p.v + base_ + ((i + rot_) & 15) * 1024; asm volatile("global_load_dwordx4 %0, %1, %2" PV_POL : "=v"(vr[i]) : "v"(lo_), "s"(b_) : "memory"); }
    const char* src; int c;
    if (bid < 48) { src = p.wq; c = bid; } else if (bid < 96) { src = p.wo; c = bid - 48; } else if (bid < 150) { src = p.wg; c = bid - 96; }
    else if (bid < 204) { src = p.wu; c = bid - 150; } else { src = p.wd; c = bid - 204; }
#pragma unroll
    for (int i = 0; i < 12; ++i) { const char* b_ = src + (size_t)c * 98304 + wave * 12288 + ((i + rot_) % 12) * 1024; asm volatile("global_load_dwordx4 %0, %1, %2" PW_POL : "=v"(wr[i]) : "v"(lo_), "s"(b_) : "memory"); }
    VMCNT(0);
#pragma unroll
    for (int i = 0; i < 16; ++i) asm volatile("" :: "v"(kr[i]), "v"(vr[i]));
#pragma unroll
    for (int i = 0; i < 12; ++i) asm volatile("" :: "v"(wr[i]));
    STAMP(11);
    while ((u32)__builtin_amdgcn_s_memrealtime() - stamps[0] < 4000) __builtin_amdgcn_s_sleep(1);
  }
  u32 ep;
  { const u32 z = 0; asm volatile("global_load_dword %0, %1, %2 sc1" : "=v"(ep) : "v"(z), "s"(p.ws) : "memory"); }

  if (wave < 4) {
    const size_t reg = ((size_t)(bat * 16 + hk) * 4096 + 3072 + half * 512 + wave * 128) * 256;
    const char* kb = p.k + reg;
    const char* vb = p.v + reg;
    const u32 rot = (((u32)(bid * 4 + wave) * 2654435761u) >> 20) & 7;
    const u32 koff = n * 256 + g * 16, voff = g * 1024 + n * 16;
    v4u K[32], V[32];
#define LDK(t) do { const char* b_ = kb + (((t) + rot) & 7) * 4096; \
    asm volatile("global_load_dwordx4 %0, %4, %5" KV_POL "\n\tglobal_load_dwordx4 %1, %4, %5 offset:64" KV_POL "\n\t" \
                 "global_load_dwordx4 %2, %4, %5 offset:128" KV_POL "\n\tglobal_load_dwordx4 %3, %4, %5 offset:192" KV_POL \
                 : "=&v"(K[4 * (t)]), "=&v"(K[4 * (t) + 1]), "=&v"(K[4 * (t) + 2]), "=&v"(K[4 * (t) + 3]) : "v"(koff), "s"(b_) : "memory"); } while (0)
#define LDV(t) do { const char* b_ = vb + (((t) + rot) & 7) * 4096; \
    asm volatile("global_load_dwordx4 %0, %4, %5" V_POL "\n\tglobal_load_dwordx4 %1, %4, %5 offset:256" V_POL "\n\t" \
                 "global_load_dwordx4 %2, %4, %5 offset:512" V_POL "\n\tglobal_load_dwordx4 %3, %4, %5 offset:768" V_POL \
                 : "=&v"(V[4 * (t)]), "=&v"(V[4 * (t) + 1]), "=&v"(V[4 * (t) + 2]), "=&v"(V[4 * (t) + 3]) : "v"(voff), "s"(b_) : "memory"); } while (0)
#define TIE4(A, t) asm volatile("" : "+v"(A[4 * (t)]), "+v"(A[4 * (t) + 1]), "+v"(A[4 * (t) + 2]), "+v"(A[4 * (t) + 3]))
    LDK(0); LDK(1); LDK(2);
    u32 T;
    asm volatile("s_waitcnt vmcnt(12)\n\tv_readfirstlane_b32 %0, %1\n\ts_nop 1" : "=s"(T) : "v"(ep) : "memory");
    T += 1u;
    __builtin_amdgcn_s_barrier();
    asm volatile("" ::: "memory");
    STAMP(1);
    LDK(3); LDK(4); LDK(5); LDK(6); LDK(7);
    LDV(0); LDV(1); LDV(2); LDV(3);
    STAMP(2);
    lds_wait_ge(lbase + C_Q, 1);
    STAMP(3);
    v4u qf[4];
#pragma unroll
    for (int ks = 0; ks < 4; ++ks) qf[ks] = *(LAS v4u*)(lds + L_Q + ((n >> 3) & 1) * 256 + ks * 64 + g * 16);
    v4f s[8];
    VMCNT(32);
    TIE4(K, 0); TIE4(K, 1); TIE4(K, 2); TIE4(K, 3);
#pragma unroll
    for (int t = 0; t < 4; ++t) {
      s[t] = v4f{0.f, 0.f, 0.f, 0.f};
#pragma unroll
      for (int ks = 0; ks < 4; ++ks) s[t] = mfma(K[4 * t + ks], qf[ks], s[t]);
    }
    asm volatile("s_nop 7\n\ts_nop 1" : "+v"(s[0]), "+v"(s[1]), "+v"(s[2]), "+v"(s[3]));
    LDV(4); LDV(5); LDV(6); LDV(7);
    lds_inc(lbase + C_VI);
    STAMP(4);
    VMCNT(32);
    TIE4(K, 4); TIE4(K, 5); TIE4(K, 6); TIE4(K, 7);
#pragma unroll
    for (int t = 4; t < 8; ++t) {
      s[t] = v4f{0.f, 0.f, 0.f, 0.f};
#pragma unroll
      for (int ks = 0; ks < 4; ++ks) s[t] = mfma(K[4 * t + ks], qf[ks], s[t]);
    }
    constexpr float C2 = 0.08838834764831845f * 1.4426950408889634f;
    float mx = s[0][0];
#pragma unroll
    for (int t = 0; t < 8; ++t)
#pragma unroll
      for (int r = 0; r < 4; ++r) mx = fmaxf(mx, s[t][r]);
    mx = xg_max(mx) * C2;
    float ls = 0.f;
#pragma unroll
    for (int t = 0; t < 8; ++t)
#pragma unroll
      for (int r = 0; r < 4; ++r) { s[t][r] = __builtin_amdgcn_exp2f(fmaf(s[t][r], C2, -mx)); ls += s[t][r]; }
    ls = xg_sum(ls);
    if ((lane & 0x37) == 0) *(LAS v2f*)(lds + L_ML + wave * 16 + (lane >> 3) * 8) = v2f{mx, ls};
    lds_inc(lbase + C_ML);
    u32 msk[4];
#pragma unroll
    for (int i = 0; i < 4; ++i) msk[i] = (((lane & 7) >> 1) == i) ? ((lane & 1) ? 0xffff0000u : 0x0000ffffu) : 0u;
    v4f acc = {0.f, 0.f, 0.f, 0.f};
    VMCNT(16);
    TIE4(V, 0); TIE4(V, 1); TIE4(V, 2); TIE4(V, 3);
#pragma unroll
    for (int i = 0; i < 16; ++i) {
      const u32 pp = pk_bf16(s[i >> 2][i & 3], s[i >> 2][i & 3]);
      acc = mfma(V[i], v4u{pp & msk[0], pp & msk[1], pp & msk[2], pp & msk[3]}, acc);
    }
    v4u pb[16];
#pragma unroll
    for (int i = 0; i < 16; ++i) {
      const u32 pp = pk_bf16(s[4 + (i >> 2)][i & 3], s[4 + (i >> 2)][i & 3]);
      pb[i] = v4u{pp & msk[0], pp & msk[1], pp & msk[2], pp & msk[3]};
    }
    lds_wait_ge(lbase + C_ML, 4);
    float fw;
    {
      v2f ml[4];
#pragma unroll
      for (int w = 0; w < 4; ++w) ml[w] = *(LAS v2f*)(lds + L_ML + w * 16 + ((n >> 3) & 1) * 8);
      const float M = fmaxf(fmaxf(ml[0].x, ml[1].x), fmaxf(ml[2].x, ml[3].x));
      float L = 0.f;
#pragma unroll
      for (int w = 0; w < 4; ++w) L = fmaf(ml[w].y, __builtin_amdgcn_exp2f(ml[w].x - M), L);
      fw = __builtin_amdgcn_exp2f(mx - M) / L;
    }
    TIE4(pb, 0); TIE4(pb, 1); TIE4(pb, 2); TIE4(pb, 3);
    asm volatile("" : "+v"(fw), "+v"(acc));
    STAMP(5);
    VMCNT(12); TIE4(V, 4);
#pragma unroll
    for (int i = 0; i < 4; ++i) acc = mfma(V[16 + i], pb[i], acc);
    asm volatile("" : "+v"(acc));
    VMCNT(8); TIE4(V, 5);
#pragma unroll
    for (int i = 4; i < 8; ++i) acc = mfma(V[16 + i], pb[i], acc);
    asm volatile("" : "+v"(acc));
    VMCNT(4); TIE4(V, 6);
#pragma unroll
    for (int i = 8; i < 12; ++i) acc = mfma(V[16 + i], pb[i], acc);
    asm volatile("" : "+v"(acc));
    VMCNT(2); asm volatile("" : "+v"(V[28]), "+v"(V[29]));
    acc = mfma(V[28], pb[12], acc); acc = mfma(V[29], pb[13], acc);
    asm volatile("" : "+v"(acc));
    VMCNT(0); asm volatile("" : "+v"(V[30]), "+v"(V[31]));
    acc = mfma(V[30], pb[14], acc); acc = mfma(V[31], pb[15], acc);
    STAMP(6);
    acc *= fw;
    {
      const u32 a_own = lbase + L_OS + wave * 1024 + lane * 16, a_all = lbase + L_OS + lane * 16, a_cnt = lbase + C_O, one = 1;
      u64 keep; u32 tk; v4f o0, o1, o2, o3;
      asm volatile("ds_write_b128 %6, %7\n\t"
                   "s_mov_b64 %0, exec\n\ts_mov_b64 exec, 1\n\tds_add_rtn_u32 %1, %8, %9\n\ts_mov_b64 exec, %0\n\t"
                   "ds_read_b128 %2, %10\n\tds_read_b128 %3, %10 offset:1024\n\tds_read_b128 %4, %10 offset:2048\n\tds_read_b128 %5, %10 offset:3072\n\t"
                   "s_waitcnt lgkmcnt(0)"
                   : "=&s"(keep), "=&v"(tk), "=&v"(o0), "=&v"(o1), "=&v"(o2), "=&v"(o3)
                   : "v"(a_own), "v"(acc), "v"(a_cnt), "v"(one), "v"(a_all) : "memory");
      if (__builtin_amdgcn_readfirstlane(tk) == 3) {
        const v4f o = (o0 + o1) + (o2 + o3);
        const u32 off = WS_O + (2 * hk + ((n >> 3) & 1)) * 8192 + (2 * bat + half) * 512 + (8 * g + (n & 7)) * 16;
        st16_dev(p.ws, off, v4u{pk_bf16(o[0], o[1]), T, pk_bf16(o[2], o[3]), T});
      }
    }
    STAMP(7);
    if (wave == 0 || (wave == 1 && bid < 32)) {
      const int tau = wave == 0 ? bid : 256 + bid;
      const u32 po = WS_P + (lane >> 3) * P_ROW + tau * 128 + (lane & 7) * 16;
      const u32 go = WS_GATE + (lane & 3) * 16;
      const u32 po1 = po + 8 * P_ROW, po2 = po + 16 * P_ROW, po3 = po + 24 * P_ROW, po4 = po + 32 * P_ROW, po5 = po + 40 * P_ROW;
      bool first = true;
      _Pragma("nounroll") for (u32 spins = 0; spins < SPIN_CAP; ++spins) {
        v4u a0, a1, a2, a3, a4, a5, gg;
        asm volatile("global_load_dwordx4 %0, %7, %14 sc1\n\tglobal_load_dwordx4 %1, %8, %14 sc1\n\tglobal_load_dwordx4 %2, %9, %14 sc1\n\t"
                     "global_load_dwordx4 %3, %10, %14 sc1\n\tglobal_load_dwordx4 %4, %11, %14 sc1\n\tglobal_load_dwordx4 %5, %12, %14 sc1\n\t"
                     "global_load_dwordx4 %6, %13, %14 sc1\n\ts_waitcnt vmcnt(0)"
                     : "=&v"(a0), "=&v"(a1), "=&v"(a2), "=&v"(a3), "=&v"(a4), "=&v"(a5), "=&v"(gg)
                     : "v"(po), "v"(po1), "v"(po2), "v"(po3), "v"(po4), "v"(po5), "v"(go), "s"(p.ws) : "memory");
        const u32 bad = (a0.y ^ T) | (a0.w ^ T) | (a1.y ^ T) | (a1.w ^ T) | (a2.y ^ T) | (a2.w ^ T) | (a3.y ^ T) | (a3.w ^ T) |
                        (a4.y ^ T) | (a4.w ^ T) | (a5.y ^ T) | (a5.w ^ T) | (gg.y ^ T) | (gg.w ^ T);
        const bool ok = __builtin_amdgcn_ballot_w64(bad != 0) == 0;
        if (ok || first) {
          first = false;
#define F_(v) __uint_as_float(v)
          const float g0 = F_(gg.x), g1 = F_(gg.z);
          float v0 = g0 * ((F_(a0.x) + F_(a1.x)) + (F_(a2.x) + F_(a3.x))) + (1.f - g0) * (F_(a4.x) + F_(a5.x));
          float v1 = g1 * ((F_(a0.z) + F_(a1.z)) + (F_(a2.z) + F_(a3.z))) + (1.f - g1) * (F_(a4.z) + F_(a5.z));
          v0 += dpp<ROR8>(v0); v1 += dpp<ROR8>(v1);
          v0 = xg_sum(v0); v1 = xg_sum(v1);
          const float w0 = dpp<ROR4>(v0), w1 = dpp<ROR4>(v1);
          const bool hi = (lane & 4) != 0;
          const u32 r = hi ? pk_bf16(w1, v1) : pk_bf16(v0, w0);
          if (ok) {
            if (lane < 8) *(u32*)(p.out + ((2 * (lane & 3) + (hi ? 1 : 0)) * 576 + 2 * tau) * 2) = r;
            if (tau == 0 && lane == 0) __hip_atomic_store((u32*)(p.ws + WS_EPOCH), T, __ATOMIC_RELAXED, __HIP_MEMORY_SCOPE_AGENT);
            break;
          }
          asm volatile("" :: "v"(r));
        }
        __builtin_amdgcn_s_sleep(1);
      }
      STAMP(8);
    }
    STAMP_FLUSH();
    return;
  }

  const int hw = wave - 4;
  __builtin_amdgcn_s_setprio(3);
  const int drow = lane >> 3, dch = (lane & 7) ^ ((drow >> 1) & 3);
  const bool isS = jc >= 26, isQ = jc >= 18 && !isS;
  const int unit = isS ? xc * 6 + (jc - 26) : isQ ? xc * 8 + (jc - 18) : xc * 18 + jc;
  const char* t1src = nullptr; size_t t1step = 0;
  if (!isS) {
#pragma unroll
    for (int i = 0; i < 3; ++i) { const int c = min(hw + 4 * i, 8); DMA16(p.x + c * 1024 + lane * 16, lbase + L_X + c * 1024, ""); }
    if (isQ) { t1src = p.wq + (size_t)(144 * hw + drow) * 8192 + unit * 128 + dch * 16; t1step = 8 * 8192; }
    else { t1src = (dch < 4 ? p.wg : p.wu) + (size_t)(144 * hw + drow) * 9216 + unit * 64 + (dch & 3) * 16; t1step = 8 * 9216; }
#pragma unroll
    for (int i = 0; i < 8; ++i) { DMA16(t1src, lbase + L_T1 + (18 * hw + i) * 1024, W_POL); t1src += t1step; }
    VMCNT(11);
  } else {
    VMCNT(0);
  }
  u32 T;
  asm volatile("v_readfirstlane_b32 %0, %1\n\ts_nop 1" : "=s"(T) : "v"(ep) : "memory");
  T += 1u;
  if (hw == 0 && lane < 16) *(LAS u32*)(lds + L_CTL + lane * 4) = 0u;
  asm volatile("s_waitcnt lgkmcnt(0)" ::: "memory");
  __builtin_amdgcn_s_barrier();
  asm volatile("" ::: "memory");
  STAMP(1);
  const int beta = 2 * (n >> 2) + (n & 1);
  if (!isS) {
#pragma unroll
    for (int i = 8; i < 18; ++i) { DMA16(t1src, lbase + L_T1 + (18 * hw + i) * 1024, W_POL); t1src += t1step; }
    VMCNT(0);
    lds_inc(lbase + C_S1);
    lds_wait_ge(lbase + C_S1, 4);
    STAMP(2);
    const u32 ta = L_T1 + tr_lane_addr(lane, hw);
    const u32 xa = L_X + beta * 1152 + g * 8;
    v4f acc = {0.f, 0.f, 0.f, 0.f};
#pragma unroll
    for (int ks = 0; ks < 18; ++ks) {
      const v2u x0 = *(LAS v2u*)(lds + xa + ks * 64), x1 = *(LAS v2u*)(lds + xa + ks * 64 + 32);
      acc = mfma(v4u{x0.x, x0.y, x1.x, x1.y}, trfrag(lds, ta, ks * 4096), acc);
    }
    if (isQ) {
      const float n0 = dpp<XOR1>(acc[0]), n1 = dpp<XOR1>(acc[1]);
      const u32 pk = (n & 1) ? pk_bf16(n1, acc[1]) : pk_bf16(acc[0], n0);
      const int col = 64 * unit + (n >> 3) * 32 + 8 * hw + (n & 7);
      st8_dev(p.ws, WS_Q + ((2 * g + (n & 1)) * 2048 + (col >> 1)) * 8, v2u{pk, T});
    } else {
      const float o0 = dpp<ROR8>(acc[0]), o1 = dpp<ROR8>(acc[1]);
      const float gv = bf16r(n < 8 ? acc[0] : o1), uv = bf16r(n < 8 ? o0 : acc[1]);
      const float sg = bf16r(gv / (1.f + __expf(-gv)));
      const float hv = sg * uv, hn = dpp<XOR1>(hv);
      const int col = 32 * unit + 8 * hw + (n & 7);
      if ((n & 1) == 0) st8_dev(p.ws, WS_H + ((2 * g + (n >> 3)) * 2304 + (col >> 1)) * 8, v2u{pk_bf16(hv, hn), T});
    }
    STAMP(3);
  }

  if (hw == 3) {
    if (isS && unit == 0) {
      const v4u* xp = (const v4u*)(p.x + (lane >> 3) * 1152) + (lane & 7);
      const v4u* gp = (const v4u*)p.gw + (lane & 7);
      float sum = 0.f;
#pragma unroll
      for (int i = 0; i < 9; ++i) {
        const v4u a = xp[8 * i], b = gp[8 * i];
#pragma unroll
        for (int e = 0; e < 4; ++e) {
          sum = fmaf(__uint_as_float(a[e] << 16), __uint_as_float(b[e] << 16), sum);
          sum = fmaf(__uint_as_float(a[e] & 0xffff0000u), __uint_as_float(b[e] & 0xffff0000u), sum);
        }
      }
      sum += dpp<XOR1>(sum); sum += dpp<XOR2>(sum); sum += dpp<HMIR>(sum);
      const float gt = 1.f / (1.f + __expf(-sum));
      if ((lane & 7) == 0) st8_dev(p.ws, WS_GATE + (lane >> 3) * 8, v2u{__float_as_uint(gt), T});
    }
    const u32 qo = WS_Q + (bat * 2048 + hk * 128) * 8 + lane * 16;
    v4u qv;
    _Pragma("nounroll") for (u32 spins = 0; spins < SPIN_CAP; ++spins) {
      asm volatile("global_load_dwordx4 %0, %1, %2 sc1\n\ts_waitcnt vmcnt(0)" : "=&v"(qv) : "v"(qo), "s"(p.ws) : "memory");
      if (__builtin_amdgcn_ballot_w64(((qv.y ^ T) | (qv.w ^ T)) != 0) == 0) break;
      __builtin_amdgcn_s_sleep(4);
    }
    *(LAS v2u*)(lds + L_Q + lane * 8) = v2u{qv.x, qv.z};
    lds_inc(lbase + C_Q);
    STAMP(4);
    lds_wait_ge(lbase + C_ML, 4);
    if (lane < 2) {
      v2f ml[4];
#pragma unroll
      for (int w = 0; w < 4; ++w) ml[w] = *(LAS v2f*)(lds + L_ML + w * 16 + lane * 8);
      const float M = fmaxf(fmaxf(ml[0].x, ml[1].x), fmaxf(ml[2].x, ml[3].x));
      float L = 0.f;
#pragma unroll
      for (int w = 0; w < 4; ++w) L = fmaf(ml[w].y, __builtin_amdgcn_exp2f(ml[w].x - M), L);
      st8_dev(p.ws, WS_LSE + ((2 * hk + lane) * 16 + 2 * bat + half) * 8, v2u{__float_as_uint(M + __builtin_amdgcn_logf(L)), T});
    }
    STAMP(5);
    STAMP_FLUSH();
    return;
  }

  const u32 pst = ((n & 7) + (n >> 3) * 32) * 64 + g * 16;
  if (isS) {
    const int rg = unit / 3, cg = 3 * (unit % 3) + hw;
    v4u A[9];
    {
      const u32 ho = WS_H + (beta * 2304 + rg * 144) * 8 + g * 16;
      _Pragma("nounroll") for (u32 spins = 0; spins < SPIN_CAP; ++spins) {
        v4u h[18];
#define LH(i) "global_load_dwordx4 %" #i ", %18, %19 offset:" #i "*64 sc1\n\t"
        asm volatile(LH(0) LH(1) LH(2) LH(3) LH(4) LH(5) LH(6) LH(7) LH(8) LH(9) LH(10) LH(11) LH(12) LH(13) LH(14) LH(15) LH(16) LH(17) "s_waitcnt vmcnt(0)"
                     : "=&v"(h[0]), "=&v"(h[1]), "=&v"(h[2]), "=&v"(h[3]), "=&v"(h[4]), "=&v"(h[5]), "=&v"(h[6]), "=&v"(h[7]), "=&v"(h[8]),
                       "=&v"(h[9]), "=&v"(h[10]), "=&v"(h[11]), "=&v"(h[12]), "=&v"(h[13]), "=&v"(h[14]), "=&v"(h[15]), "=&v"(h[16]), "=&v"(h[17])
                     : "v"(ho), "s"(p.ws) : "memory");
        u32 bad = 0;
#pragma unroll
        for (int i = 0; i < 18; ++i) bad |= (h[i].y ^ T) | (h[i].w ^ T);
#pragma unroll
        for (int ks = 0; ks < 9; ++ks) A[ks] = v4u{h[2 * ks].x, h[2 * ks].z, h[2 * ks + 1].x, h[2 * ks + 1].z};
        if (__builtin_amdgcn_ballot_w64(bad != 0) == 0) break;
        __builtin_amdgcn_s_sleep(32);
      }
    }
    STAMP(4);
    lds_wait_ge(lbase + C_VI, 4);
    STAMP(5);
    const char* src = p.wd + (size_t)(288 * rg + drow) * 1152 + cg * 128 + dch * 16;
    const u32 tb = L_T1 + hw * 36864;
#pragma unroll
    for (int i = 0; i < 36; ++i) { DMA16(src, lbase + tb + i * 1024, W_POL); src += 8 * 1152; }
    STAMP(6);
    u32 ta[4];
    v4f acc[4];
#pragma unroll
    for (int j = 0; j < 4; ++j) { ta[j] = tb + tr_lane_addr(lane, j); acc[j] = v4f{0.f, 0.f, 0.f, 0.f}; }
#define DSTEP(ks, N) do { VMCNT(N); _Pragma("unroll") for (int j = 0; j < 4; ++j) acc[j] = mfma(A[ks], trfrag(lds, ta[j], (ks) * 4096), acc[j]); } while (0)
    DSTEP(0, 32); DSTEP(1, 28); DSTEP(2, 24); DSTEP(3, 20); DSTEP(4, 16); DSTEP(5, 12); DSTEP(6, 8); DSTEP(7, 4); DSTEP(8, 0);
    const u32 po = WS_P + (32 + rg) * P_ROW + cg * 4096 + pst;
#pragma unroll
    for (int j = 0; j < 4; ++j) st16_dev(p.ws, po + j * 512, v4u{__float_as_uint(acc[j][0]), T, __float_as_uint(acc[j][1]), T});
    STAMP(8);
    STAMP_FLUSH();
    return;
  }
  if (hw == 0 || (hw == 1 && jc < 10)) {
    const int task = hw == 0 ? jc : 26 + jc, head = 4 * xc + task / 9, cg = task % 9;
    lds_wait_ge(lbase + C_VI, 4);
    STAMP(4);
    const char* src = p.wo + (size_t)(128 * head + 8 * (drow & 3) + (drow >> 2)) * 1152 + cg * 128 + dch * 16;
    const u32 tb = L_WO + hw * 16384;
#pragma unroll
    for (int i = 0; i < 16; ++i) { const char* s_ = src + (32 * (i >> 2) + 2 * (i & 3)) * 1152; DMA16(s_, lbase + tb + i * 1024, W_POL); }
    float w0, w1, w2, w3;
    {
      const u32 lo = WS_LSE + (head * 16 + 4 * g) * 8;
      v4u e0, e1;
      _Pragma("nounroll") for (u32 spins = 0; spins < SPIN_CAP; ++spins) {
        asm volatile("global_load_dwordx4 %0, %2, %3 sc1\n\tglobal_load_dwordx4 %1, %2, %3 offset:16 sc1\n\ts_waitcnt vmcnt(0)"
                     : "=&v"(e0), "=&v"(e1) : "v"(lo), "s"(p.ws) : "memory");
        if (__builtin_amdgcn_ballot_w64(((e0.y ^ T) | (e0.w ^ T) | (e1.y ^ T) | (e1.w ^ T)) != 0) == 0) break;
        __builtin_amdgcn_s_sleep(2);
      }
      const float d0 = __uint_as_float(e0.z) - __uint_as_float(e0.x), d1 = __uint_as_float(e1.z) - __uint_as_float(e1.x);
      w0 = 1.f / (1.f + __builtin_amdgcn_exp2f(d0)); w1 = 1.f / (1.f + __builtin_amdgcn_exp2f(-d0));
      w2 = 1.f / (1.f + __builtin_amdgcn_exp2f(d1)); w3 = 1.f / (1.f + __builtin_amdgcn_exp2f(-d1));
    }
    STAMP(5);
    v4u B[16];
#pragma unroll
    for (int j = 0; j < 4; ++j) {
      const u32 ta = tb + tr_lane_addr(lane, j);
#pragma unroll
      for (int ks = 0; ks < 4; ++ks) B[4 * j + ks] = trfrag(lds, ta, ks * 4096);
    }
    TIE4(B, 0); TIE4(B, 1); TIE4(B, 2); TIE4(B, 3);
    STAMP(6);
    const u32 oo = WS_O + head * 8192 + n * 512 + g * 16;
    const u32 po = WS_P + head * P_ROW + cg * 4096 + pst;
    bool first = true;
    _Pragma("nounroll") for (u32 spins = 0; spins < SPIN_CAP; ++spins) {
      v4u u[8];
#define LO(i) "global_load_dwordx4 %" #i ", %8, %9 offset:" #i "*64 sc1\n\t"
      asm volatile(LO(0) LO(1) LO(2) LO(3) LO(4) LO(5) LO(6) LO(7) "s_waitcnt vmcnt(0)"
                   : "=&v"(u[0]), "=&v"(u[1]), "=&v"(u[2]), "=&v"(u[3]), "=&v"(u[4]), "=&v"(u[5]), "=&v"(u[6]), "=&v"(u[7])
                   : "v"(oo), "s"(p.ws) : "memory");
      u32 bad = 0;
#pragma unroll
      for (int i = 0; i < 8; ++i) bad |= (u[i].y ^ T) | (u[i].w ^ T);
      const bool ok = __builtin_amdgcn_ballot_w64(bad != 0) == 0;
      if (ok || first) {
        first = false;
        v4f acc[4];
#pragma unroll
        for (int j = 0; j < 4; ++j) acc[j] = v4f{0.f, 0.f, 0.f, 0.f};
#pragma unroll
        for (int ks = 0; ks < 4; ++ks) {
          const v4u A = {u[2 * ks].x, u[2 * ks].z, u[2 * ks + 1].x, u[2 * ks + 1].z};
#pragma unroll
          for (int j = 0; j < 4; ++j) acc[j] = mfma(A, B[4 * j + ks], acc[j]);
        }
        v4u r[4];
#pragma unroll
        for (int j = 0; j < 4; ++j)
          r[j] = v4u{__float_as_uint(fmaf(w0, acc[j][0], w1 * acc[j][1])), T, __float_as_uint(fmaf(w2, acc[j][2], w3 * acc[j][3])), T};
        if (ok) {
#pragma unroll
          for (int j = 0; j < 4; ++j) st16_dev(p.ws, po + j * 512, r[j]);
          break;
        }
        asm volatile("" :: "v"(r[0]), "v"(r[1]), "v"(r[2]), "v"(r[3]));
      }
      __builtin_amdgcn_s_sleep(1);
    }
    STAMP(8);
  }
  STAMP_FLUSH();
}

extern "C" void kernel_launch(void* const* d_in, const int* in_sizes, int n_in, void* d_out, int out_size, void* d_ws, size_t ws_size,
                              hipStream_t stream) {
  Params p;
  p.x = (const char*)d_in[0]; p.k = (const char*)d_in[1]; p.v = (const char*)d_in[2]; p.wq = (const char*)d_in[3]; p.wo = (const char*)d_in[4];
  p.wg = (const char*)d_in[6]; p.wu = (const char*)d_in[7]; p.wd = (const char*)d_in[8]; p.gw = (const char*)d_in[9];
  p.out = (char*)d_out; p.ws = (char*)d_ws;
  hipLaunchKernelGGL(fused_decode, dim3(256), dim3(512), 0, stream, p);
}
```

```cpp
#define PACE_DT 58
#define M1 160
#define D2 110
#include <hip/hip_runtime.h>
#include <stdint.h>

typedef unsigned u32;
typedef unsigned long long u64;
typedef u32 v4u __attribute__((ext_vector_type(4)));
typedef u32 v2u __attribute__((ext_vector_type(2)));
typedef float v4f __attribute__((ext_vector_type(4)));
typedef float v2f __attribute__((ext_vector_type(2)));
typedef short v4s __attribute__((ext_vector_type(4)));
typedef __bf16 v8bf __attribute__((ext_vector_type(8)));
typedef __bf16 v2bf __attribute__((ext_vector_type(2)));
#define LAS __attribute__((address_space(3)))
#define DEVINL __device__ __forceinline__
#define NOUNROLL _Pragma("nounroll")
#define KNOB(name, val) name
#define OLAY 1
#define PACE_ORG 300
#define SLOW_SLEEP 32
#define Q_AT 620
#define H_AT 700
#define LSE_WAIT 80
#define ST_AT 420
#define LATE_PEN 40
#define H_GAP "6"
#define S1_DT 57
#define S1_ORG 40
#define M1_OWN 30
#define S2_DELAY 30
#define S3_DELAY 30
#define PD_AT 110
#define ROTM 0
#define TRICKLE 0
#define TR_AT 450
#define GRAN3 0
#define EARLY 0
#define PRET 0
#define S2_GAP 14
#define S3_GAP 8

constexpr u32 WS_EPOCH = 0;
constexpr u32 WS_GATE = 256;
constexpr u32 WS_ST = 512;
constexpr u32 WS_LSE = 4096;
constexpr u32 WS_A1 = WS_LSE + 32 * 16 * 64;
constexpr u32 A1_SG = 64 * 2048, A1_UP = (64 + 72) * 2048;
constexpr u32 WS_O = WS_A1 + 208 * 2048;
constexpr u32 WS_P = WS_O + 32 * 16 * 512;
constexpr u32 P_ROW = 288 * 64;
constexpr u32 WS_DIAG = 4u << 20;
constexpr u32 L_T1 = 0;
constexpr u32 L_X = 73728;
constexpr u32 L_WO = L_X + 9216;
constexpr u32 L_HA = 110592;
constexpr u32 L_Q = 119808;
constexpr u32 L_ML = L_Q + 512;
constexpr u32 L_CTL = L_ML + 64;
constexpr u32 L_OS = L_CTL + 64;
constexpr u32 L_W = L_OS + 4096;
constexpr u32 L_END = L_W + 256;
constexpr u32 C_S1 = L_CTL + 0, C_Q = L_CTL + 4, C_ML = L_CTL + 8, C_O = L_CTL + 12, C_VI = L_CTL + 16, C_H = L_CTL + 20, C_X = L_CTL + 24, C_W = L_CTL + 28, C_T = L_CTL + 32, C_X1 = L_CTL + 36, C_ORG = L_CTL + 40;
constexpr u32 SPIN_CAP = 1u << 17;

struct Params {
  const char* x; const char* k; const char* v; const char* wq; const char* wo; const char* wg; const char* wu; const char* wd;
  const char* gw; char* out; char* ws;
};

DEVINL v4f mfma(v4u a, v4u b, v4f c) {
  return __builtin_amdgcn_mfma_f32_16x16x32_bf16(__builtin_bit_cast(v8bf, a), __builtin_bit_cast(v8bf, b), c, 0, 0, 0);
}
DEVINL u32 pk_bf16(float lo, float hi) { v2f f = {lo, hi}; return __builtin_bit_cast(u32, __builtin_convertvector(f, v2bf)); }
DEVINL float bf16r(float f) { return __builtin_bit_cast(float, pk_bf16(f, f) & 0xffff0000u); }
DEVINL float lo_f(u32 v) { return __uint_as_float(v << 16); }
DEVINL float hi_f(u32 v) { return __uint_as_float(v & 0xffff0000u); }
DEVINL float rcp(float x) { return __builtin_amdgcn_rcpf(x); }
template <int CTRL> DEVINL float dpp(float x) {
  return __builtin_bit_cast(float, __builtin_amdgcn_mov_dpp(__builtin_bit_cast(int, x), CTRL, 0xf, 0xf, true));
}
constexpr int XOR1 = 0xB1, XOR2 = 0x4E, ROR4 = 0x124, ROR8 = 0x128, HMIR = 0x141;
DEVINL float xg_max(float x) {
  auto s = __builtin_amdgcn_permlane16_swap(__float_as_uint(x), __float_as_uint(x), false, false);
  x = fmaxf(__uint_as_float(s[0]), __uint_as_float(s[1]));
  auto t = __builtin_amdgcn_permlane32_swap(__float_as_uint(x), __float_as_uint(x), false, false);
  return fmaxf(__uint_as_float(t[0]), __uint_as_float(t[1]));
}
DEVINL float xg_sum(float x) {
  auto s = __builtin_amdgcn_permlane16_swap(__float_as_uint(x), __float_as_uint(x), false, false);
  x = __uint_as_float(s[0]) + __uint_as_float(s[1]);
  auto t = __builtin_amdgcn_permlane32_swap(__float_as_uint(x), __float_as_uint(x), false, false);
  return __uint_as_float(t[0]) + __uint_as_float(t[1]);
}
DEVINL u32 tagbad(u32 acc, v4u u, u32 T) {
  acc = __builtin_amdgcn_bitop3_b32(acc, u.y, T, 0xF6);
  return __builtin_amdgcn_bitop3_b32(acc, u.w, T, 0xF6);
}
DEVINL u64 ballot_nz(u32 v) { return __builtin_amdgcn_ballot_w64(v != 0); }
DEVINL void lds_inc(u32 addr) {
  const u32 one = 1;
  asm volatile("s_mov_b64 exec, 1\n\tds_add_u32 %0, %1\n\ts_mov_b64 exec, -1" :: "v"(addr), "v"(one) : "memory");
}
template <int SLEEP> DEVINL void lds_wait_ge(u32 addr, u32 want) {
  NOUNROLL for (u32 spins = 0; spins < (SPIN_CAP << 4); ++spins) {
    u32 r;
    asm volatile("ds_read_b32 %0, %1\n\ts_waitcnt lgkmcnt(0)" : "=v"(r) : "v"(addr) : "memory");
    if ((u32)__builtin_amdgcn_readfirstlane(r) >= want) break;
    if (SLEEP) __builtin_amdgcn_s_sleep(SLEEP);
  }
}
DEVINL u32 lds_word(u32 addr) {
  u32 r;
  asm volatile("ds_read_b32 %0, %1\n\ts_waitcnt lgkmcnt(0)" : "=v"(r) : "v"(addr) : "memory");
  return __builtin_amdgcn_readfirstlane(r);
}
DEVINL u32 now() { return (u32)__builtin_amdgcn_s_memrealtime(); }
template <int SLEEP> DEVINL void wait_until(u32 t) {
  NOUNROLL while ((int)(now() - t) < 0) { if (SLEEP) __builtin_amdgcn_s_sleep(SLEEP); }
}
#define DMA16(voff, sbase, ldsdst, POL) \
  asm volatile("s_mov_b32 m0, %2\n\ts_nop 0\n\tglobal_load_lds_dwordx4 %0, %1" POL :: "v"(voff), "s"(sbase), "s"(ldsdst) : "memory")
#define VMCNT_(n) asm volatile("s_waitcnt vmcnt(" #n ")" ::: "memory")
#define VMCNT(n) VMCNT_(n)
DEVINL void st16_dev(char* base, u32 off, v4u d) { asm volatile("global_store_dwordx4 %0, %1, %2 sc1\n\ts_nop 1" :: "v"(off), "v"(d), "s"(base) : "memory"); }
DEVINL void st8_dev(char* base, u32 off, v2u d) { asm volatile("global_store_dwordx2 %0, %1, %2 sc1" :: "v"(off), "v"(d), "s"(base) : "memory"); }
DEVINL v4u trfrag(LAS char* l, u32 a0, u32 imm) {
  const v2u x = __builtin_bit_cast(v2u, __builtin_amdgcn_ds_read_tr16_b64_v4i16((LAS v4s*)(l + a0 + imm)));
  const v2u y = __builtin_bit_cast(v2u, __builtin_amdgcn_ds_read_tr16_b64_v4i16((LAS v4s*)(l + a0 + imm + 2048)));
  return v4u{x.x, x.y, y.x, y.y};
}
DEVINL u32 tr_lane_addr(int lane, int j) {
  const int r0 = (lane >> 2) & 15, p = lane & 3;
  const int c = (p < 2 ? j : j + 4) ^ ((r0 >> 1) & 3);
  return r0 * 128 + c * 16 + 8 * (p & 1);
}

#define STAMP(i)
#define STAMP_DECL
#define STAMP_FLUSH()

__global__ void __launch_bounds__(512, 2) fused_decode(Params p) {
  __shared__ __attribute__((aligned(1024))) char lds_raw[L_END];
  LAS char* lds = (LAS char*)lds_raw;
  const u32 lbase = (u32)(uintptr_t)lds;
  const int tid = threadIdx.x, lane = tid & 63, wave = __builtin_amdgcn_readfirstlane(tid >> 6);
  const int bid = blockIdx.x, xc = bid & 7, jc = bid >> 3;
  const int hk = 2 * xc + (jc & 1), half = (jc >> 1) & 1, bat = jc >> 2;
  const int g = lane >> 4, n = lane & 15;
  STAMP_DECL;
  STAMP(0);
  const u32 t_start = (u32)__builtin_amdgcn_s_memrealtime();
  {
    const char* pc; u32 len;
    asm volatile("s_getpc_b64 %0\n.Lfd_anchor:\n\ts_mov_b32 %1, .Lfd_end-.Lfd_anchor" : "=s"(pc), "=s"(len));
    const u32 po_ = (u32)(jc * 8 + wave) * 1024u;
    if (po_ < len) { const u32 lo_ = lane * 16; DMA16(lo_, (const char*)((uintptr_t)(pc + min(po_, len - 1024u)) & ~(uintptr_t)15), lbase + L_WO + wave * 1024, ""); }
  }
  asm volatile("" :: "s"(p.x), "s"(p.k), "s"(p.v), "s"(p.wq), "s"(p.wo), "s"(p.wg), "s"(p.wu), "s"(p.wd), "s"(p.gw), "s"(p.out), "s"(p.ws));

  const bool isS = jc >= 26;
  const int kind = jc < 8 ? 0 : jc < 17 ? 1 : 2;
  const int j9 = kind == 0 ? jc : jc - (kind == 1 ? 8 : 17);
  const char* w1 = kind == 0 ? p.wq : kind == 1 ? p.wg : p.wu;
  const u32 r9 = (3u - (u32)((uintptr_t)w1 >> 7)) & 7u;
  const int unit = j9 < 8 ? xc * 8 + j9 : 64 + ((xc + r9 - (kind == 1 ? 0 : 1)) & 7);
  const bool slow = !isS && (u32)(unit & 7) == r9;

  if (wave < 4) {
    const size_t reg = ((size_t)(bat * 16 + hk) * 4096 + 3072 + half * 512 + wave * (GRAN3 ? 16 : 128)) * 256;
    const char* kb = p.k + reg;
    const char* vb = p.v + reg;
    const u32 hcu = ((u32)bid * 2654435761u) >> 20;
    const u32 rot = GRAN3 ? hcu & 7 : ROTM == 0 ? (((u32)(bid * 4 + wave) * 2654435761u) >> 20) & 7 : ROTM == 1 ? (2 * wave + jc) & 7 : ROTM == 2 ? (2 * wave + jc + xc) & 7
                    : ROTM == 3 ? (2 * wave + (jc >> 2) + jc) & 7 : (2 * wave + (jc >> 2) + 3 * xc) & 7;
    const u32 koff = (n >> 2) * 256 + g * 64 + (n & 3) * 16, voff = lane * 16;
    v4u K[32], V[32];
#define LD4(A, base, off, t) do { const char* b_ = base + (((t) + rot) & 7) * (GRAN3 ? 16384 : 4096); \
    asm volatile("global_load_dwordx4 %0, %4, %5 nt\n\tglobal_load_dwordx4 %1, %4, %5 offset:1024 nt\n\t" \
                 "global_load_dwordx4 %2, %4, %5 offset:2048 nt\n\tglobal_load_dwordx4 %3, %4, %5 offset:3072 nt" \
                 : "=&v"(A[4 * (t)]), "=&v"(A[4 * (t) + 1]), "=&v"(A[4 * (t) + 2]), "=&v"(A[4 * (t) + 3]) : "v"(off), "s"(b_) : "memory"); } while (0)
#define LDK(t) LD4(K, kb, koff, t)
#define LDV(t) LD4(V, vb, voff, t)
#define TIE4(A, t) asm volatile("" : "+v"(A[4 * (t)]), "+v"(A[4 * (t) + 1]), "+v"(A[4 * (t) + 2]), "+v"(A[4 * (t) + 3]))
    const u32 t_off = PACE_ORG + ((GRAN3 ? (hcu >> 3) & 3 : wave) * PACE_DT) / 4, a_org = lbase + C_ORG;
#define PACE(m) wait_until<1>(lds_word(a_org) + t_off + (m) * PACE_DT)
    if (!slow) { __builtin_amdgcn_s_barrier(); PACE(0); }
    asm volatile("" ::: "memory");
    STAMP(1);
    LDK(0);
    if (!slow) PACE(1);
    LDK(1);
    if (!slow) PACE(2);
    LDK(2);
    if (slow) __builtin_amdgcn_s_barrier();
    PACE(3); LDK(3); PACE(4); LDK(4); PACE(5); LDK(5); PACE(6); LDK(6); PACE(7); LDK(7);
    PACE(8); LDV(0); PACE(9); LDV(1); PACE(10); LDV(2);
    STAMP(2);
    lds_wait_ge<1>(lbase + C_Q, 1);
    STAMP(3);
    const v4u qf = *(LAS v4u*)(lds + L_Q + (n >> 3) * 256 + (4 * g + (n & 3)) * 16);
    const u32 T = *(LAS u32*)(lds + C_T);
    float s[32];
    const v4f zero = {0.f, 0.f, 0.f, 0.f};
#define QK(i) do { const v4f d_ = mfma(K[i], qf, zero); s[i] = ((dpp<0x00>(d_[0]) + dpp<0x55>(d_[1])) + dpp<0xAA>(d_[2])) + dpp<0xFF>(d_[3]); } while (0)
    VMCNT(12);
    TIE4(K, 0); TIE4(K, 1); TIE4(K, 2); TIE4(K, 3);
#pragma unroll
    for (int i = 0; i < 16; ++i) QK(i);
    TIE4(s, 0); TIE4(s, 1); TIE4(s, 2); TIE4(s, 3);
    PACE(11); LDV(3);
    TIE4(K, 4); TIE4(K, 5); TIE4(K, 6); TIE4(K, 7);
#pragma unroll
    for (int i = 16; i < 32; ++i) QK(i);
    constexpr float C2 = 0.08838834764831845f * 1.4426950408889634f;
    float mx = s[0];
#pragma unroll
    for (int i = 1; i < 32; ++i) mx = fmaxf(mx, s[i]);
    mx = xg_max(mx) * C2;
    asm volatile("" : "+v"(mx));
    PACE(12); LDV(4);
    asm volatile("" : "+v"(mx));
    float ls = 0.f;
#pragma unroll
    for (int i = 0; i < 32; ++i) { s[i] = __builtin_amdgcn_exp2f(fmaf(s[i], C2, -mx)); ls += s[i]; }
    ls = xg_sum(ls);
    if ((lane & 0x37) == 0) *(LAS v2f*)(lds + L_ML + wave * 16 + (lane >> 3) * 8) = v2f{mx, ls};
    lds_inc(lbase + C_ML);
    PACE(13); LDV(5);
    u32 msk[4];
#pragma unroll
    for (int i = 0; i < 4; ++i) msk[i] = (((lane & 7) >> 1) == i) ? ((lane & 1) ? 0xffff0000u : 0x0000ffffu) : 0u;
    v4f acc = zero;
    VMCNT(8);
    TIE4(V, 0); TIE4(V, 1); TIE4(V, 2); TIE4(V, 3);
#pragma unroll
    for (int i = 0; i < 16; ++i) {
      const u32 pp = pk_bf16(s[i], s[i]);
      acc = mfma(V[i], v4u{pp & msk[0], pp & msk[1], pp & msk[2], pp & msk[3]}, acc);
    }
    asm volatile("" : "+v"(acc));
    PACE(14); LDV(6);
    TIE4(s, 4); TIE4(s, 5); TIE4(s, 6); TIE4(s, 7);
    v4u pb[16];
#pragma unroll
    for (int i = 0; i < 16; ++i) {
      const u32 pp = pk_bf16(s[16 + i], s[16 + i]);
      pb[i] = v4u{pp & msk[0], pp & msk[1], pp & msk[2], pp & msk[3]};
    }
    TIE4(pb, 0); TIE4(pb, 1); TIE4(pb, 2); TIE4(pb, 3);
    PACE(15); LDV(7);
    lds_inc(lbase + C_VI);
    __builtin_amdgcn_s_setprio(3);
    STAMP(4);
    lds_wait_ge<1>(lbase + C_ML, 4);
    float fw;
    u32 lse;
    {
      v2f ml[4];
#pragma unroll
      for (int w = 0; w < 4; ++w) ml[w] = *(LAS v2f*)(lds + L_ML + w * 16 + (n >> 3) * 8);
      const float M = fmaxf(fmaxf(ml[0].x, ml[1].x), fmaxf(ml[2].x, ml[3].x));
      float L = 0.f;
#pragma unroll
      for (int w = 0; w < 4; ++w) L = fmaf(ml[w].y, __builtin_amdgcn_exp2f(ml[w].x - M), L);
      fw = __builtin_amdgcn_exp2f(mx - M) * rcp(L);
      lse = __float_as_uint(M + __builtin_amdgcn_logf(L));
    }
    u32 a_own = lbase + L_OS + wave * 1024 + lane * 16, a_all = lbase + L_OS + lane * 16, a_cnt = lbase + C_O, one = 1;
    u32 ooff = WS_O + (2 * hk + (n >> 3)) * 8192 + (2 * g + ((n >> 2) & 1)) * 1024 + (2 * bat + half) * 64 + (n & 3) * 16;
    u32 loff = WS_LSE + ((2 * hk + (n >> 3)) * 16 + 2 * bat + half) * 64 + (n & 3) * 16;
    v4u lsev = {lse, T, lse, T};
    asm volatile("" : "+v"(fw), "+v"(acc), "+v"(a_own), "+v"(a_all), "+v"(a_cnt), "+v"(one), "+v"(ooff), "+v"(loff), "+v"(lsev));
    STAMP(5);
    VMCNT(12); TIE4(V, 4);
#pragma unroll
    for (int i = 0; i < 4; ++i) acc = mfma(V[16 + i], pb[i], acc);
    asm volatile("" : "+v"(acc));
    VMCNT(8); TIE4(V, 5);
#pragma unroll
    for (int i = 4; i < 8; ++i) acc = mfma(V[16 + i], pb[i], acc);
    asm volatile("" : "+v"(acc));
    VMCNT(4); TIE4(V, 6);
#pragma unroll
    for (int i = 8; i < 12; ++i) acc = mfma(V[16 + i], pb[i], acc);
    asm volatile("" : "+v"(acc));
    VMCNT(2); asm volatile("" : "+v"(V[28]), "+v"(V[29]));
    acc = mfma(V[28], pb[12], acc); acc = mfma(V[29], pb[13], acc);
    asm volatile("" : "+v"(acc));
    VMCNT(0); asm volatile("" : "+v"(V[30]), "+v"(V[31]));
    acc = mfma(V[30], pb[14], acc); acc = mfma(V[31], pb[15], acc);
    STAMP(6);
    acc *= fw;
    {
      u32 tk; v4f o0, o1, o2, o3;
      asm volatile("ds_write_b128 %5, %6\n\t"
                   "s_mov_b64 exec, 1\n\tds_add_rtn_u32 %0, %7, %8\n\ts_mov_b64 exec, -1\n\t"
                   "ds_read_b128 %1, %9\n\tds_read_b128 %2, %9 offset:1024\n\tds_read_b128 %3, %9 offset:2048\n\tds_read_b128 %4, %9 offset:3072\n\t"
                   "s_waitcnt lgkmcnt(0)"
                   : "=&v"(tk), "=&v"(o0), "=&v"(o1), "=&v"(o2), "=&v"(o3)
                   : "v"(a_own), "v"(acc), "v"(a_cnt), "v"(one), "v"(a_all) : "memory");
      if (__builtin_amdgcn_readfirstlane(tk) == 3) {
        const v4f o = (o0 + o1) + (o2 + o3);
        asm volatile("s_mov_b64 exec, 0x0f0f\n\tglobal_store_dwordx4 %0, %1, %2 sc1\n\ts_mov_b64 exec, -1" :: "v"(loff), "v"(lsev), "s"(p.ws) : "memory");
        st16_dev(p.ws, ooff, v4u{pk_bf16(o[0], o[1]), T, pk_bf16(o[2], o[3]), T});
      }
    }
    STAMP(7);
    STAMP_FLUSH();
    asm volatile(".Lfd_end:");
    return;
  }

  const int hw = wave - 4;
  __builtin_amdgcn_s_setprio(3);
  u32 ep;
  { const u32 z = 0; asm volatile("global_load_dword %0, %1, %2 sc1" : "=v"(ep) : "v"(z), "s"(p.ws) : "memory"); }
  const int drow = lane >> 3, dch = (lane & 7) ^ ((drow >> 1) & 3);
    {
      const int sl = jc >> 3;
      if (sl < 3 || xc < 2) {
        const char* m_ = sl == 0 ? p.wq : (sl == 2 || (sl == 3 && xc == 1)) ? p.wu : p.wg;
        const u32 rowb = sl == 0 ? 8192 : 9216;
        const u32 un = (sl == 3 ? 64 : 8 * xc) + ((3u - (u32)((uintptr_t)m_ >> 7)) & 7u);
        const u32 o_ = (lane >> 3) * rowb + (lane & 7) * 16;
        const char* b_ = m_ + (u32)(144 * hw + 16 + 8 * (jc & 7)) * rowb + un * 128;
        DMA16(o_, b_, lbase + L_OS + hw * 1024, "");
        DMA16(o_, b_ + 64 * rowb, lbase + L_OS + hw * 1024, "");
      }
    }
  if (hw == 0 && lane < 16) *(LAS u32*)(lds + L_CTL + lane * 4) = lane == 10 ? t_start : 0u;
  if (!isS) {
    const u32 xo = (lane & 7) * 1152 + (lane >> 3) * 16;
#pragma unroll
    for (int i = 0; i < 3; ++i) { const int c = min(hw + 4 * i, 8); DMA16(xo, p.x + c * 128, lbase + L_X + c * 1024, ""); }
  }
  const int beta = 2 * (n >> 2) + (n & 1);
  const int us = xc * 6 + (jc - 26), rg = us / 3;
  {
    const u32 rowb = isS ? 1152 : kind == 0 ? 8192 : 9216;
    const char* src = isS ? p.wd + (size_t)(288 * rg + 144 * (hw & 1)) * 1152 + (3 * (us % 3) + (hw >> 1)) * 128
                          : w1 + (size_t)(144 * hw) * rowb + unit * 128;
    const u32 so = drow * rowb + dch * 16;
    u32 dst = lbase + L_T1 + 18 * hw * 1024;
    if (slow) __builtin_amdgcn_s_sleep(SLOW_SLEEP);
    u32 tq = t_start * 4 + S1_ORG * 4;
    int n1 = 18;
    NOUNROLL for (int i = 0; i < n1; ++i) {
      if (!slow) { NOUNROLL while ((int)(now() * 4 - tq) < 0) __builtin_amdgcn_s_sleep(1); }
      tq += S1_DT;
      DMA16(so, src, dst, " nt"); src += 8 * rowb; dst += 1024; }
    (void)tq;
  }
  asm volatile("s_waitcnt lgkmcnt(0)" ::: "memory");
  __builtin_amdgcn_s_barrier();
  asm volatile("" ::: "memory");
  STAMP(1);
  VMCNT(18);
  u32 T;
  asm volatile("v_readfirstlane_b32 %0, %1\n\ts_nop 1" : "=s"(T) : "v"(ep) : "memory");
  T += 1u;
  lds_inc(lbase + C_X);
  if (bid < 8 && hw == 3 && lane == 0) st8_dev(p.ws, WS_ST + bid * 8, v2u{t_start, T});
  v4u xf[18];
  if (!isS) {
    lds_wait_ge<1>(lbase + C_X, 4);
    u32 xa = L_X + (g >> 1) * 128 + beta * 16 + (g & 1) * 8;
    asm volatile("" : "+v"(xa));
#pragma unroll
    for (int ks = 0; ks < 18; ++ks) {
      const v2u x0 = *(LAS v2u*)(lds + xa + ks * 512), x1 = *(LAS v2u*)(lds + xa + ks * 512 + 256);
      xf[ks] = v4u{x0.x, x0.y, x1.x, x1.y};
    }
  }
  VMCNT(0);
  lds_inc(lbase + C_S1);
  if (!isS) {
    u32 ta = L_T1 + tr_lane_addr(lane, hw), tb2 = ta + 9 * 4096;
    asm volatile("" : "+v"(ta), "+v"(tb2));
    lds_wait_ge<1>(lbase + C_S1, 4);
    STAMP(2);
    v4u bf[18];
#pragma unroll
    for (int ks = 0; ks < 18; ++ks) bf[ks] = ks < 9 ? trfrag(lds, ta, ks * 4096) : trfrag(lds, tb2, (ks - 9) * 4096);
    __builtin_amdgcn_sched_barrier(0);
    v4f acc = {0.f, 0.f, 0.f, 0.f}, acc2 = {0.f, 0.f, 0.f, 0.f};
#pragma unroll
    for (int ks = 0; ks < 18; ks += 2) { acc = mfma(xf[ks], bf[ks], acc); acc2 = mfma(xf[ks + 1], bf[ks + 1], acc2); }
    acc += acc2;
    float a0 = acc[0], a1 = acc[1];
    if (kind == 1) {
      a0 = bf16r(a0); a1 = bf16r(a1);
      a0 = a0 * rcp(1.f + __expf(-a0)); a1 = a1 * rcp(1.f + __expf(-a1));
    }
    const float n0 = dpp<XOR1>(a0), n1 = dpp<XOR1>(a1);
    const u32 pk = (n & 1) ? pk_bf16(n1, a1) : pk_bf16(a0, n0);
    const u32 pk2 = __builtin_bit_cast(u32, dpp<XOR2>(__builtin_bit_cast(float, pk)));
    const u32 ao = WS_A1 + (kind == 0 ? 0 : kind == 1 ? A1_SG : A1_UP) + (unit * 4 + hw) * 512 + (2 * g + (n & 1)) * 64 + (n >> 2) * 16;
    if ((n & 2) == 0) st16_dev(p.ws, ao, v4u{pk, T, pk2, T});
    STAMP(3);
  }

  if (hw == 2 && bid == 0) {
    const v4u* gp = (const v4u*)p.gw + (lane & 7);
    v4u gv[9];
#pragma unroll
    for (int i = 0; i < 9; ++i) gv[i] = gp[8 * i];
    float sum = 0.f;
#pragma unroll
    for (int i = 0; i < 9; ++i) {
      const v4u a = *(LAS v4u*)(lds + L_X + (lane & 7) * 128 + (lane >> 3) * 16 + i * 1024);
#pragma unroll
      for (int e = 0; e < 4; ++e) { sum = fmaf(lo_f(a[e]), lo_f(gv[i][e]), sum); sum = fmaf(hi_f(a[e]), hi_f(gv[i][e]), sum); }
    }
    sum += dpp<XOR1>(sum); sum += dpp<XOR2>(sum); sum += dpp<HMIR>(sum);
    const float gt = rcp(1.f + __expf(-sum));
    if ((lane & 7) == 0) st8_dev(p.ws, WS_GATE + (lane >> 3) * 8, v2u{__float_as_uint(gt), T});
  }
  const int ntask = isS ? 0 : jc < 10 ? 2 : 1;
  if (hw == 3) {
    if (isS) lds_wait_ge<1>(lbase + C_S1, 4);
    {
      const u32 so_ = WS_ST + (lane & 3) * 16;
      v4u sv;
      wait_until<4>(t_start + ST_AT);
      asm volatile("global_load_dwordx4 %0, %1, %2 sc1\n\ts_waitcnt vmcnt(0)" : "=&v"(sv) : "v"(so_), "s"(p.ws) : "memory");
      float dsum = (float)(int)(sv.x - t_start) + (float)(int)(sv.z - t_start);
      dsum += dpp<XOR1>(dsum); dsum += dpp<XOR2>(dsum);
      if (ballot_nz(tagbad(0u, sv, T)) == 0) *(LAS u32*)(lds + C_ORG) = t_start + (u32)(int)fminf(fmaxf(dsum * 0.125f, -200.f), 200.f);
    }
    {
      const u32 qo = WS_A1 + (16 * hk + (lane >> 2)) * 512 + bat * 64 + (lane & 3) * 16, go = WS_GATE + (lane & 3) * 16;
      v4u qv, gg;
      wait_until<4>(t_start + Q_AT);
      NOUNROLL for (u32 spins = 0; spins < SPIN_CAP; ++spins) {
        asm volatile("global_load_dwordx4 %0, %2, %4 sc1\n\tglobal_load_dwordx4 %1, %3, %4 sc1\n\ts_waitcnt vmcnt(0)" : "=&v"(qv), "=&v"(gg) : "v"(qo), "v"(go), "s"(p.ws) : "memory");
        if (ballot_nz(tagbad(tagbad(0u, qv, T), gg, T)) == 0) break;
        __builtin_amdgcn_s_sleep(8);
      }
      *(LAS v2u*)(lds + L_Q + (lane >> 4) * 128 + ((lane >> 1) & 1) * 64 + ((lane >> 2) & 3) * 16 + (lane & 1) * 8) = v2u{qv.x, qv.z};
      *(LAS v2u*)(lds + L_W + 128 + (lane & 3) * 8) = v2u{gg.x, gg.z};
      *(LAS u32*)(lds + C_T) = T;
      lds_inc(lbase + C_Q);
    }
    STAMP(4);
    STAMP_FLUSH();
    return;
  }

  const u32 pst = n * 64 + g * 16;
  if (isS) {
    const int cg = 3 * (us % 3) + hw;
    const u32 tb = L_T1 + hw * 36864;
    if (hw == 2) {
      const char* src = p.wd + (size_t)(288 * rg) * 1152 + cg * 128;
      const u32 so = drow * 1152 + dch * 16;
      u32 dst = lbase + tb;
      wait_until<8>(t_start + PACE_ORG + 14 * PACE_DT);
      lds_wait_ge<0>(lbase + C_VI, 4);
      STAMP(5);
      u32 tq = lds_word(lbase + C_ORG) + PACE_ORG + 15 * PACE_DT + (3 * PACE_DT) / 4 + S3_DELAY;
      NOUNROLL for (int i = 0; i < 9; ++i) {
        wait_until<0>(tq); tq += S3_GAP;
#pragma unroll
        for (int k = 0; k < 4; ++k) { DMA16(so, src, dst, " nt"); src += 8 * 1152; dst += 1024; }
      }
    } else {
      lds_wait_ge<2>(lbase + C_S1, 4);
      wait_until<8>(t_start + H_AT);
      const int u0 = (9 * rg) >> 1;
      const u32 ho = WS_A1 + A1_SG + u0 * 2048 + hw * 1024 + lane * 16 + 4096, uo = ho + (A1_UP - A1_SG), ho2 = ho + 4096, uo2 = uo + 4096;
      v4u h[10];
      NOUNROLL for (u32 spins = 0; spins < SPIN_CAP; ++spins) {
#define LH(i, a, o) "global_load_dwordx4 %" #i ", %" #a ", %14 offset:" #o " sc1\n\ts_sleep " H_GAP "\n\t"
        asm volatile(LH(0, 10, -4096) LH(1, 10, -2048) LH(2, 10, 0) LH(3, 10, 2048) LH(4, 12, 0)
                     LH(5, 11, -4096) LH(6, 11, -2048) LH(7, 11, 0) LH(8, 11, 2048) LH(9, 13, 0) "s_waitcnt vmcnt(0)"
                     : "=&v"(h[0]), "=&v"(h[1]), "=&v"(h[2]), "=&v"(h[3]), "=&v"(h[4]), "=&v"(h[5]), "=&v"(h[6]), "=&v"(h[7]), "=&v"(h[8]), "=&v"(h[9])
                     : "v"(ho), "v"(uo), "v"(ho2), "v"(uo2), "s"(p.ws) : "memory");
        u32 bad = 0;
#pragma unroll
        for (int i = 0; i < 10; ++i) bad = tagbad(bad, h[i], T);
        if (ballot_nz(bad) == 0) break;
        __builtin_amdgcn_s_sleep(8);
      }
#define HMUL(a, b) pk_bf16(lo_f(a) * lo_f(b), hi_f(a) * hi_f(b))
      const u32 hl = L_HA + ((lane >> 1) & 1) * 512 + (2 * (2 * hw + (lane >> 5)) + (lane & 1)) * 64 + ((lane >> 2) & 7) * 8;
#pragma unroll
      for (int k = 0; k < 5; ++k) *(LAS v2u*)(lds + hl + k * 1024) = v2u{HMUL(h[k].x, h[5 + k].x), HMUL(h[k].z, h[5 + k].z)};
      lds_inc(lbase + C_H);
    }
    STAMP(6);
    lds_wait_ge<1>(lbase + C_H, 2);
    v4u A[9];
    {
      u32 ha = L_HA + (rg & 1) * 512 + g * 64 + beta * 8;
      asm volatile("" : "+v"(ha));
#pragma unroll
      for (int ks = 0; ks < 9; ++ks) {
        const v2u a0 = *(LAS v2u*)(lds + ha + ks * 512), a1 = *(LAS v2u*)(lds + ha + ks * 512 + 256);
        A[ks] = v4u{a0.x, a0.y, a1.x, a1.y};
      }
    }
    u32 ta[4];
    v4f acc[4];
#pragma unroll
    for (int j = 0; j < 4; ++j) { ta[j] = tb + tr_lane_addr(lane, j); acc[j] = v4f{0.f, 0.f, 0.f, 0.f}; }
    v4u Bc[4], Bn[4];
#define DREAD(D, ks) _Pragma("unroll") for (int j = 0; j < 4; ++j) D[j] = trfrag(lds, ta[j], (ks) * 4096)
#define DMMA(D, ks) _Pragma("unroll") for (int j = 0; j < 4; ++j) acc[j] = mfma(A[ks], D[j], acc[j])
#define DSTEP(CUR, NXT, ks, N) do { VMCNT(N); DREAD(NXT, (ks) + 1); __builtin_amdgcn_sched_barrier(0); DMMA(CUR, ks); __builtin_amdgcn_sched_barrier(0); } while (0)
    VMCNT(32); DREAD(Bc, 0);
    DSTEP(Bc, Bn, 0, 28); DSTEP(Bn, Bc, 1, 24); DSTEP(Bc, Bn, 2, 20); DSTEP(Bn, Bc, 3, 16); DSTEP(Bc, Bn, 4, 12); DSTEP(Bn, Bc, 5, 8); DSTEP(Bc, Bn, 6, 4); DSTEP(Bn, Bc, 7, 0);
    DMMA(Bc, 8);
    const u32 po = WS_P + (32 + rg) * P_ROW + cg * 2048 + pst;
    if (PD_AT) { asm volatile("" : "+v"(acc[0]), "+v"(acc[1]), "+v"(acc[2]), "+v"(acc[3])); wait_until<4>(lds_word(lbase + C_ORG) + PACE_ORG + 15 * PACE_DT + (3 * PACE_DT) / 4 + PD_AT); }
    st16_dev(p.ws, po, v4u{pk_bf16(acc[0][0], acc[0][1]), T, pk_bf16(acc[1][0], acc[1][1]), T});
    st16_dev(p.ws, po + 1024, v4u{pk_bf16(acc[2][0], acc[2][1]), T, pk_bf16(acc[3][0], acc[3][1]), T});
    STAMP(8);
    STAMP_FLUSH();
    return;
  }
  if (hw < ntask) {
    const int task = hw == 0 ? jc : 26 + jc, head = 4 * xc + task / 9, cg = task % 9, tau = xc * 36 + task;
    const u32 tb = L_WO + hw * 16384;
    {
      const char* src = p.wo + (size_t)(128 * head) * 1152 + cg * 128;
      const u32 so = (8 * (drow & 3) + (drow >> 2)) * 1152 + dch * 16;
      u32 dst = lbase + tb;
      wait_until<8>(t_start + PACE_ORG + 14 * PACE_DT);
      lds_wait_ge<0>(lbase + C_VI, 4);
      STAMP(4);
      u32 tq = lds_word(lbase + C_ORG) + PACE_ORG + 15 * PACE_DT + (3 * PACE_DT) / 4 + S2_DELAY;
      NOUNROLL for (int i = 0; i < 4; ++i) {
        wait_until<0>(tq); tq += S2_GAP;
#pragma unroll
        for (int k = 0; k < 4; ++k) { DMA16(so, src + 2 * k * 1152, dst, " nt"); dst += 1024; }
        src += 32 * 1152;
      }
    }
    u32 oo = WS_O + head * 8192 + n * 64 + g * 16, oo2 = oo + 4096;
#define LO(i, a, o) "global_load_dwordx4 %" #i ", %" #a ", %12 offset:" #o "*1024 sc1\n\t"
    u32 po = WS_P + head * P_ROW + cg * 2048 + pst;
    u32 ro0 = WS_P + (lane >> 2) * P_ROW + tau * 64 + (lane & 3) * 16, ro1 = ro0 + 16 * P_ROW, ro2 = ro0 + 32 * P_ROW;
    const int tl = tau & 31;
    u32 outo = ((2 * (lane & 3) + ((lane >> 2) & 1)) * 576 + (tau >> 5) * 64 + ((tl >> 3) & 1) * 32 + (tl >> 4) * 16 + (tl & 7) + ((lane >> 3) & 1) * 8) * 2;
    u32 ta[4];
#pragma unroll
    for (int j = 0; j < 4; ++j) ta[j] = tb + tr_lane_addr(lane, j);
    asm volatile("" : "+v"(oo), "+v"(oo2), "+v"(po), "+v"(ro0), "+v"(ro1), "+v"(ro2), "+v"(outo), "+v"(ta[0]), "+v"(ta[1]), "+v"(ta[2]), "+v"(ta[3]));
    v2f gv = *(LAS v2f*)(lds + L_W + 128 + (lane & 3) * 8);
    u32 lo = WS_LSE + (head * 16 + 4 * g + (n & 3)) * 64;
    asm volatile("" : "+v"(gv), "+v"(lo));
    const u32 xs = lds_word(lbase + C_ORG) + PACE_ORG + 15 * PACE_DT + (3 * PACE_DT) / 4 + M1;
    lds_wait_ge<0>(lbase + C_O, 4);
    u32 x1 = now() + M1_OWN;
    if ((int)(x1 - xs) < 0) x1 = xs;
    u32 tries = 0;
    {
      v4u u[8], B[16];
      v2u e;
#define SWEEP_O(C, PRE, POST) asm volatile(PRE "global_load_dwordx2 %8, %11, %12 sc1\n\t" LO(0, 9, 0) LO(1, 9, 1) LO(2, 9, 2) LO(3, 9, 3) LO(4, 10, 0) LO(5, 10, 1) LO(6, 10, 2) LO(7, 10, 3) POST \
                     : C(u[0]), C(u[1]), C(u[2]), C(u[3]), C(u[4]), C(u[5]), C(u[6]), C(u[7]), C(e) \
                     : "v"(oo), "v"(oo2), "v"(lo), "s"(p.ws), "s"(need) : "memory")
      u64 need = ~0ull;
      wait_until<0>(x1);
      STAMP(5);
      SWEEP_O("=&v", "", "s_waitcnt vmcnt(9)");
      STAMP(9);
#pragma unroll
      for (int j = 0; j < 4; ++j)
#pragma unroll
        for (int ks = 0; ks < 4; ++ks) B[4 * j + ks] = trfrag(lds, ta[j], ks * 4096);
      asm volatile("s_waitcnt vmcnt(0)" : "+v"(u[0]), "+v"(u[1]), "+v"(u[2]), "+v"(u[3]), "+v"(u[4]), "+v"(u[5]), "+v"(u[6]), "+v"(u[7]), "+v"(e) :: "memory");
      NOUNROLL for (u32 spins = 0; spins < SPIN_CAP; ++spins) {
        u32 bad = e.y ^ T;
#pragma unroll
        for (int i = 0; i < 8; ++i) bad = tagbad(bad, u[i], T);
        need = ballot_nz(bad);
        if (need == 0) break;
        tries += 10;
        SWEEP_O("+v", "s_sleep 2\n\ts_mov_b64 exec, %13\n\t", "s_mov_b64 exec, -1\n\ts_waitcnt vmcnt(0)");
      }
      STAMP(6);
      const float me = __uint_as_float(e.x), wme = rcp(1.f + __builtin_amdgcn_exp2f(dpp<XOR1>(me) - me));
      const v4f wv = {dpp<0x00>(wme), dpp<0x55>(wme), dpp<0xAA>(wme), dpp<0xFF>(wme)};
      v4f acc[4];
#pragma unroll
      for (int j = 0; j < 4; ++j) acc[j] = v4f{0.f, 0.f, 0.f, 0.f};
#pragma unroll
      for (int ks = 0; ks < 4; ++ks) {
        const v4u A = {u[2 * ks].x, u[2 * ks].z, u[2 * ks + 1].x, u[2 * ks + 1].z};
#pragma unroll
        for (int j = 0; j < 4; ++j) acc[j] = mfma(A, B[4 * j + ks], acc[j]);
      }
#define MIX(j) pk_bf16(fmaf(wv[0], acc[j][0], wv[1] * acc[j][1]), fmaf(wv[2], acc[j][2], wv[3] * acc[j][3]))
      st16_dev(p.ws, po, v4u{MIX(0), T, MIX(1), T});
      st16_dev(p.ws, po + 1024, v4u{MIX(2), T, MIX(3), T});
    }
    STAMP(7);
    {
      v4u a[3];
      u64 nd[3];
#define LP(i, o, m) "s_mov_b64 exec, %" #m "\n\tglobal_load_dwordx4 %" #i ", %" #o ", %6 sc1\n\t"
#pragma unroll
      for (int i = 0; i < 3; ++i) { a[i] = v4u{0u, 0u, 0u, 0u}; nd[i] = ~0ull; }
      wait_until<0>(x1 + D2);
      STAMP(10);
      NOUNROLL for (u32 spins = 0; spins < SPIN_CAP; ++spins) {
        asm volatile(LP(0, 3, 7) LP(1, 4, 8) LP(2, 5, 9) "s_mov_b64 exec, -1\n\ts_waitcnt vmcnt(0)"
                     : "+v"(a[0]), "+v"(a[1]), "+v"(a[2]) : "v"(ro0), "v"(ro1), "v"(ro2), "s"(p.ws), "s"(nd[0]), "s"(nd[1]), "s"(nd[2]) : "memory");
#pragma unroll
        for (int i = 0; i < 3; ++i) nd[i] = ballot_nz(tagbad(0u, a[i], T));
        if ((nd[0] | nd[1] | nd[2]) == 0) break;
        tries += 1;
        __builtin_amdgcn_s_sleep(2);
      }
      const float v00 = gv[0] * (lo_f(a[0].x) + lo_f(a[1].x)) + (1.f - gv[0]) * lo_f(a[2].x), v01 = gv[1] * (hi_f(a[0].x) + hi_f(a[1].x)) + (1.f - gv[1]) * hi_f(a[2].x);
      const float v10 = gv[0] * (lo_f(a[0].z) + lo_f(a[1].z)) + (1.f - gv[0]) * lo_f(a[2].z), v11 = gv[1] * (hi_f(a[0].z) + hi_f(a[1].z)) + (1.f - gv[1]) * hi_f(a[2].z);
      const bool b2 = (lane & 4) != 0, b3 = (lane & 8) != 0;
      const float c0 = (b2 ? v01 : v00) + dpp<ROR4>(b2 ? v00 : v01), c1 = (b2 ? v11 : v10) + dpp<ROR4>(b2 ? v10 : v11);
      const float r = xg_sum((b3 ? c1 : c0) + dpp<ROR8>(b3 ? c0 : c1));
      if (lane < 16) *(unsigned short*)(p.out + outo) = (unsigned short)pk_bf16(r, r);
      if (tau == 0 && lane == 0) __hip_atomic_store((u32*)(p.ws + WS_EPOCH), T, __ATOMIC_RELAXED, __HIP_MEMORY_SCOPE_AGENT);
    }
    STAMP(8);
    (void)tries;
  }
  STAMP_FLUSH();
}

extern "C" void kernel_launch(void* const* d_in, const int* in_sizes, int n_in, void* d_out, int out_size, void* d_ws, size_t ws_size,
                              hipStream_t stream) {
  Params p;
  p.x = (const char*)d_in[0]; p.k = (const char*)d_in[1]; p.v = (const char*)d_in[2]; p.wq = (const char*)d_in[3]; p.wo = (const char*)d_in[4];
  p.wg = (const char*)d_in[6]; p.wu = (const char*)d_in[7]; p.wd = (const char*)d_in[8]; p.gw = (const char*)d_in[9];
  p.out = (char*)d_out; p.ws = (char*)d_ws;
  hipLaunchKernelGGL(fused_decode, dim3(256), dim3(512), 0, stream, p);
}
```

```cpp
#define PACE_DT 58 M1=160 D2=110 PROBE_SIGN=1
#define PROBE_ST -1
#define PROBE_COND 1
#include <hip/hip_runtime.h>
#include <stdint.h>

typedef unsigned u32;
typedef unsigned long long u64;
typedef u32 v4u __attribute__((ext_vector_type(4)));
typedef u32 v2u __attribute__((ext_vector_type(2)));
typedef float v4f __attribute__((ext_vector_type(4)));
typedef float v2f __attribute__((ext_vector_type(2)));
typedef short v4s __attribute__((ext_vector_type(4)));
typedef __bf16 v8bf __attribute__((ext_vector_type(8)));
typedef __bf16 v2bf __attribute__((ext_vector_type(2)));
#define LAS __attribute__((address_space(3)))
#define DEVINL __device__ __forceinline__
#define NOUNROLL _Pragma("nounroll")
#define KNOB(name, val) name
#define OLAY 1
#define PACE_ORG 300
#define SLOW_SLEEP 32
#define Q_AT 620
#define H_AT 700
#define LSE_WAIT 80
#define M1 160
#define D2 110
#define ST_AT 420
#define LATE_PEN 40
#define H_GAP "6"
#define S1_DT 57
#define S1_ORG 40
#define M1_OWN 30
#define S2_DELAY 30
#define S3_DELAY 30
#define PD_AT 110
#define ROTM 0
#define TRICKLE 0
#define TR_AT 450
#define GRAN3 0
#define EARLY 0
#define PRET 0
#define S2_GAP 14
#define S3_GAP 8

constexpr u32 WS_EPOCH = 0;
constexpr u32 WS_GATE = 256;
constexpr u32 WS_ST = 512;
constexpr u32 WS_LSE = 4096;
constexpr u32 WS_A1 = WS_LSE + 32 * 16 * 64;
constexpr u32 A1_SG = 64 * 2048, A1_UP = (64 + 72) * 2048;
constexpr u32 WS_O = WS_A1 + 208 * 2048;
constexpr u32 WS_P = WS_O + 32 * 16 * 512;
constexpr u32 P_ROW = 288 * 64;
constexpr u32 WS_DIAG = 4u << 20;
constexpr u32 L_T1 = 0;
constexpr u32 L_X = 73728;
constexpr u32 L_WO = L_X + 9216;
constexpr u32 L_HA = 110592;
constexpr u32 L_Q = 119808;
constexpr u32 L_ML = L_Q + 512;
constexpr u32 L_CTL = L_ML + 64;
constexpr u32 L_OS = L_CTL + 64;
constexpr u32 L_W = L_OS + 4096;
constexpr u32 L_END = L_W + 256;
constexpr u32 C_S1 = L_CTL + 0, C_Q = L_CTL + 4, C_ML = L_CTL + 8, C_O = L_CTL + 12, C_VI = L_CTL + 16, C_H = L_CTL + 20, C_X = L_CTL + 24, C_W = L_CTL + 28, C_T = L_CTL + 32, C_X1 = L_CTL + 36, C_ORG = L_CTL + 40;
constexpr u32 SPIN_CAP = 1u << 17;

struct Params {
  const char* x; const char* k; const char* v; const char* wq; const char* wo; const char* wg; const char* wu; const char* wd;
  const char* gw; char* out; char* ws;
};

DEVINL v4f mfma(v4u a, v4u b, v4f c) {
  return __builtin_amdgcn_mfma_f32_16x16x32_bf16(__builtin_bit_cast(v8bf, a), __builtin_bit_cast(v8bf, b), c, 0, 0, 0);
}
DEVINL u32 pk_bf16(float lo, float hi) { v2f f = {lo, hi}; return __builtin_bit_cast(u32, __builtin_convertvector(f, v2bf)); }
DEVINL float bf16r(float f) { return __builtin_bit_cast(float, pk_bf16(f, f) & 0xffff0000u); }
DEVINL float lo_f(u32 v) { return __uint_as_float(v << 16); }
DEVINL float hi_f(u32 v) { return __uint_as_float(v & 0xffff0000u); }
DEVINL float rcp(float x) { return __builtin_amdgcn_rcpf(x); }
template <int CTRL> DEVINL float dpp(float x) {
  return __builtin_bit_cast(float, __builtin_amdgcn_mov_dpp(__builtin_bit_cast(int, x), CTRL, 0xf, 0xf, true));
}
constexpr int XOR1 = 0xB1, XOR2 = 0x4E, ROR4 = 0x124, ROR8 = 0x128, HMIR = 0x141;
DEVINL float xg_max(float x) {
  auto s = __builtin_amdgcn_permlane16_swap(__float_as_uint(x), __float_as_uint(x), false, false);
  x = fmaxf(__uint_as_float(s[0]), __uint_as_float(s[1]));
  auto t = __builtin_amdgcn_permlane32_swap(__float_as_uint(x), __float_as_uint(x), false, false);
  return fmaxf(__uint_as_float(t[0]), __uint_as_float(t[1]));
}
DEVINL float xg_sum(float x) {
  auto s = __builtin_amdgcn_permlane16_swap(__float_as_uint(x), __float_as_uint(x), false, false);
  x = __uint_as_float(s[0]) + __uint_as_float(s[1]);
  auto t = __builtin_amdgcn_permlane32_swap(__float_as_uint(x), __float_as_uint(x), false, false);
  return __uint_as_float(t[0]) + __uint_as_float(t[1]);
}
DEVINL u32 tagbad(u32 acc, v4u u, u32 T) {
  acc = __builtin_amdgcn_bitop3_b32(acc, u.y, T, 0xF6);
  return __builtin_amdgcn_bitop3_b32(acc, u.w, T, 0xF6);
}
DEVINL u64 ballot_nz(u32 v) { return __builtin_amdgcn_ballot_w64(v != 0); }
DEVINL void lds_inc(u32 addr) {
  const u32 one = 1;
  asm volatile("s_mov_b64 exec, 1\n\tds_add_u32 %0, %1\n\ts_mov_b64 exec, -1" :: "v"(addr), "v"(one) : "memory");
}
template <int SLEEP> DEVINL void lds_wait_ge(u32 addr, u32 want) {
  NOUNROLL for (u32 spins = 0; spins < (SPIN_CAP << 4); ++spins) {
    u32 r;
    asm volatile("ds_read_b32 %0, %1\n\ts_waitcnt lgkmcnt(0)" : "=v"(r) : "v"(addr) : "memory");
    if ((u32)__builtin_amdgcn_readfirstlane(r) >= want) break;
    if (SLEEP) __builtin_amdgcn_s_sleep(SLEEP);
  }
}
DEVINL u32 lds_word(u32 addr) {
  u32 r;
  asm volatile("ds_read_b32 %0, %1\n\ts_waitcnt lgkmcnt(0)" : "=v"(r) : "v"(addr) : "memory");
  return __builtin_amdgcn_readfirstlane(r);
}
DEVINL u32 now() { return (u32)__builtin_amdgcn_s_memrealtime(); }
template <int SLEEP> DEVINL void wait_until(u32 t) {
  NOUNROLL while ((int)(now() - t) < 0) { if (SLEEP) __builtin_amdgcn_s_sleep(SLEEP); }
}
#define DMA16(voff, sbase, ldsdst, POL) \
  asm volatile("s_mov_b32 m0, %2\n\ts_nop 0\n\tglobal_load_lds_dwordx4 %0, %1" POL :: "v"(voff), "s"(sbase), "s"(ldsdst) : "memory")
#define VMCNT_(n) asm volatile("s_waitcnt vmcnt(" #n ")" ::: "memory")
#define VMCNT(n) VMCNT_(n)
DEVINL void st16_dev(char* base, u32 off, v4u d) { asm volatile("global_store_dwordx4 %0, %1, %2 sc1\n\ts_nop 1" :: "v"(off), "v"(d), "s"(base) : "memory"); }
DEVINL void st8_dev(char* base, u32 off, v2u d) { asm volatile("global_store_dwordx2 %0, %1, %2 sc1" :: "v"(off), "v"(d), "s"(base) : "memory"); }
DEVINL v4u trfrag(LAS char* l, u32 a0, u32 imm) {
  const v2u x = __builtin_bit_cast(v2u, __builtin_amdgcn_ds_read_tr16_b64_v4i16((LAS v4s*)(l + a0 + imm)));
  const v2u y = __builtin_bit_cast(v2u, __builtin_amdgcn_ds_read_tr16_b64_v4i16((LAS v4s*)(l + a0 + imm + 2048)));
  return v4u{x.x, x.y, y.x, y.y};
}
DEVINL u32 tr_lane_addr(int lane, int j) {
  const int r0 = (lane >> 2) & 15, p = lane & 3;
  const int c = (p < 2 ? j : j + 4) ^ ((r0 >> 1) & 3);
  return r0 * 128 + c * 16 + 8 * (p & 1);
}

#define STAMP(i) do { stamps[i] = (u32)__builtin_amdgcn_s_memrealtime(); } while (0)
#define STAMP_DECL u32 stamps[12] = {0, 0, 0, 0, 0, 0, 0, 0, 0, 0, 0, 0}
#define STAMP_FLUSH() do { u32 tgt_ = 4000; \
  if (PROBE_ST >= 0 && (PROBE_COND) && stamps[PROBE_ST < 0 ? 0 : PROBE_ST]) { const u32 d_ = stamps[PROBE_ST < 0 ? 0 : PROBE_ST] - stamps[0]; tgt_ += 10 * (PROBE_SIGN > 0 ? d_ : 4000 - d_); } \
  while ((u32)__builtin_amdgcn_s_memrealtime() - stamps[0] < tgt_) __builtin_amdgcn_s_sleep(1); } while (0)

__global__ void __launch_bounds__(512, 2) fused_decode(Params p) {
  __shared__ __attribute__((aligned(1024))) char lds_raw[L_END];
  LAS char* lds = (LAS char*)lds_raw;
  const u32 lbase = (u32)(uintptr_t)lds;
  const int tid = threadIdx.x, lane = tid & 63, wave = __builtin_amdgcn_readfirstlane(tid >> 6);
  const int bid = blockIdx.x, xc = bid & 7, jc = bid >> 3;
  const int hk = 2 * xc + (jc & 1), half = (jc >> 1) & 1, bat = jc >> 2;
  const int g = lane >> 4, n = lane & 15;
  STAMP_DECL;
  STAMP(0);
  const u32 t_start = (u32)__builtin_amdgcn_s_memrealtime();
  {
    const char* pc; u32 len;
    asm volatile("s_getpc_b64 %0\n.Lfd_anchor:\n\ts_mov_b32 %1, .Lfd_end-.Lfd_anchor" : "=s"(pc), "=s"(len));
    const u32 po_ = (u32)(jc * 8 + wave) * 1024u;
    if (po_ < len) { const u32 lo_ = lane * 16; DMA16(lo_, (const char*)((uintptr_t)(pc + min(po_, len - 1024u)) & ~(uintptr_t)15), lbase + L_WO + wave * 1024, ""); }
  }
  asm volatile("" :: "s"(p.x), "s"(p.k), "s"(p.v), "s"(p.wq), "s"(p.wo), "s"(p.wg), "s"(p.wu), "s"(p.wd), "s"(p.gw), "s"(p.out), "s"(p.ws));

  const bool isS = jc >= 26;
  const int kind = jc < 8 ? 0 : jc < 17 ? 1 : 2;
  const int j9 = kind == 0 ? jc : jc - (kind == 1 ? 8 : 17);
  const char* w1 = kind == 0 ? p.wq : kind == 1 ? p.wg : p.wu;
  const u32 r9 = (3u - (u32)((uintptr_t)w1 >> 7)) & 7u;
  const int unit = j9 < 8 ? xc * 8 + j9 : 64 + ((xc + r9 - (kind == 1 ? 0 : 1)) & 7);
  const bool slow = !isS && (u32)(unit & 7) == r9;

  if (wave < 4) {
    const size_t reg = ((size_t)(bat * 16 + hk) * 4096 + 3072 + half * 512 + wave * (GRAN3 ? 16 : 128)) * 256;
    const char* kb = p.k + reg;
    const char* vb = p.v + reg;
    const u32 hcu = ((u32)bid * 2654435761u) >> 20;
    const u32 rot = GRAN3 ? hcu & 7 : ROTM == 0 ? (((u32)(bid * 4 + wave) * 2654435761u) >> 20) & 7 : ROTM == 1 ? (2 * wave + jc) & 7 : ROTM == 2 ? (2 * wave + jc + xc) & 7
                    : ROTM == 3 ? (2 * wave + (jc >> 2) + jc) & 7 : (2 * wave + (jc >> 2) + 3 * xc) & 7;
    const u32 koff = (n >> 2) * 256 + g * 64 + (n & 3) * 16, voff = lane * 16;
    v4u K[32], V[32];
#define LD4(A, base, off, t) do { const char* b_ = base + (((t) + rot) & 7) * (GRAN3 ? 16384 : 4096); \
    asm volatile("global_load_dwordx4 %0, %4, %5 nt\n\tglobal_load_dwordx4 %1, %4, %5 offset:1024 nt\n\t" \
                 "global_load_dwordx4 %2, %4, %5 offset:2048 nt\n\tglobal_load_dwordx4 %3, %4, %5 offset:3072 nt" \
                 : "=&v"(A[4 * (t)]), "=&v"(A[4 * (t) + 1]), "=&v"(A[4 * (t) + 2]), "=&v"(A[4 * (t) + 3]) : "v"(off), "s"(b_) : "memory"); } while (0)
#define LDK(t) LD4(K, kb, koff, t)
#define LDV(t) LD4(V, vb, voff, t)
#define TIE4(A, t) asm volatile("" : "+v"(A[4 * (t)]), "+v"(A[4 * (t) + 1]), "+v"(A[4 * (t) + 2]), "+v"(A[4 * (t) + 3]))
    const u32 t_off = PACE_ORG + ((GRAN3 ? (hcu >> 3) & 3 : wave) * PACE_DT) / 4, a_org = lbase + C_ORG;
#define PACE(m) wait_until<1>(lds_word(a_org) + t_off + (m) * PACE_DT)
    if (!slow) { __builtin_amdgcn_s_barrier(); PACE(0); }
    asm volatile("" ::: "memory");
    STAMP(1);
    LDK(0);
    if (!slow) PACE(1);
    LDK(1);
    if (!slow) PACE(2);
    LDK(2);
    if (slow) __builtin_amdgcn_s_barrier();
    PACE(3); LDK(3); PACE(4); LDK(4); PACE(5); LDK(5); PACE(6); LDK(6); PACE(7); LDK(7);
    PACE(8); LDV(0); PACE(9); LDV(1); PACE(10); LDV(2);
    STAMP(2);
    lds_wait_ge<1>(lbase + C_Q, 1);
    STAMP(3);
    const v4u qf = *(LAS v4u*)(lds + L_Q + (n >> 3) * 256 + (4 * g + (n & 3)) * 16);
    const u32 T = *(LAS u32*)(lds + C_T);
    float s[32];
    const v4f zero = {0.f, 0.f, 0.f, 0.f};
#define QK(i) do { const v4f d_ = mfma(K[i], qf, zero); s[i] = ((dpp<0x00>(d_[0]) + dpp<0x55>(d_[1])) + dpp<0xAA>(d_[2])) + dpp<0xFF>(d_[3]); } while (0)
    VMCNT(12);
    TIE4(K, 0); TIE4(K, 1); TIE4(K, 2); TIE4(K, 3);
#pragma unroll
    for (int i = 0; i < 16; ++i) QK(i);
    TIE4(s, 0); TIE4(s, 1); TIE4(s, 2); TIE4(s, 3);
    PACE(11); LDV(3);
    TIE4(K, 4); TIE4(K, 5); TIE4(K, 6); TIE4(K, 7);
#pragma unroll
    for (int i = 16; i < 32; ++i) QK(i);
    constexpr float C2 = 0.08838834764831845f * 1.4426950408889634f;
    float mx = s[0];
#pragma unroll
    for (int i = 1; i < 32; ++i) mx = fmaxf(mx, s[i]);
    mx = xg_max(mx) * C2;
    asm volatile("" : "+v"(mx));
    PACE(12); LDV(4);
    asm volatile("" : "+v"(mx));
    float ls = 0.f;
#pragma unroll
    for (int i = 0; i < 32; ++i) { s[i] = __builtin_amdgcn_exp2f(fmaf(s[i], C2, -mx)); ls += s[i]; }
    ls = xg_sum(ls);
    if ((lane & 0x37) == 0) *(LAS v2f*)(lds + L_ML + wave * 16 + (lane >> 3) * 8) = v2f{mx, ls};
    lds_inc(lbase + C_ML);
    PACE(13); LDV(5);
    u32 msk[4];
#pragma unroll
    for (int i = 0; i < 4; ++i) msk[i] = (((lane & 7) >> 1) == i) ? ((lane & 1) ? 0xffff0000u : 0x0000ffffu) : 0u;
    v4f acc = zero;
    VMCNT(8);
    TIE4(V, 0); TIE4(V, 1); TIE4(V, 2); TIE4(V, 3);
#pragma unroll
    for (int i = 0; i < 16; ++i) {
      const u32 pp = pk_bf16(s[i], s[i]);
      acc = mfma(V[i], v4u{pp & msk[0], pp & msk[1], pp & msk[2], pp & msk[3]}, acc);
    }
    asm volatile("" : "+v"(acc));
    PACE(14); LDV(6);
    TIE4(s, 4); TIE4(s, 5); TIE4(s, 6); TIE4(s, 7);
    v4u pb[16];
#pragma unroll
    for (int i = 0; i < 16; ++i) {
      const u32 pp = pk_bf16(s[16 + i], s[16 + i]);
      pb[i] = v4u{pp & msk[0], pp & msk[1], pp & msk[2], pp & msk[3]};
    }
    TIE4(pb, 0); TIE4(pb, 1); TIE4(pb, 2); TIE4(pb, 3);
    PACE(15); LDV(7);
    lds_inc(lbase + C_VI);
    __builtin_amdgcn_s_setprio(3);
    STAMP(4);
    lds_wait_ge<1>(lbase + C_ML, 4);
    float fw;
    u32 lse;
    {
      v2f ml[4];
#pragma unroll
      for (int w = 0; w < 4; ++w) ml[w] = *(LAS v2f*)(lds + L_ML + w * 16 + (n >> 3) * 8);
      const float M = fmaxf(fmaxf(ml[0].x, ml[1].x), fmaxf(ml[2].x, ml[3].x));
      float L = 0.f;
#pragma unroll
      for (int w = 0; w < 4; ++w) L = fmaf(ml[w].y, __builtin_amdgcn_exp2f(ml[w].x - M), L);
      fw = __builtin_amdgcn_exp2f(mx - M) * rcp(L);
      lse = __float_as_uint(M + __builtin_amdgcn_logf(L));
    }
    u32 a_own = lbase + L_OS + wave * 1024 + lane * 16, a_all = lbase + L_OS + lane * 16, a_cnt = lbase + C_O, one = 1;
    u32 ooff = WS_O + (2 * hk + (n >> 3)) * 8192 + (2 * g + ((n >> 2) & 1)) * 1024 + (2 * bat + half) * 64 + (n & 3) * 16;
    u32 loff = WS_LSE + ((2 * hk + (n >> 3)) * 16 + 2 * bat + half) * 64 + (n & 3) * 16;
    v4u lsev = {lse, T, lse, T};
    asm volatile("" : "+v"(fw), "+v"(acc), "+v"(a_own), "+v"(a_all), "+v"(a_cnt), "+v"(one), "+v"(ooff), "+v"(loff), "+v"(lsev));
    STAMP(5);
    VMCNT(12); TIE4(V, 4);
#pragma unroll
    for (int i = 0; i < 4; ++i) acc = mfma(V[16 + i], pb[i], acc);
    asm volatile("" : "+v"(acc));
    VMCNT(8); TIE4(V, 5);
#pragma unroll
    for (int i = 4; i < 8; ++i) acc = mfma(V[16 + i], pb[i], acc);
    asm volatile("" : "+v"(acc));
    VMCNT(4); TIE4(V, 6);
#pragma unroll
    for (int i = 8; i < 12; ++i) acc = mfma(V[16 + i], pb[i], acc);
    asm volatile("" : "+v"(acc));
    VMCNT(2); asm volatile("" : "+v"(V[28]), "+v"(V[29]));
    acc = mfma(V[28], pb[12], acc); acc = mfma(V[29], pb[13], acc);
    asm volatile("" : "+v"(acc));
    VMCNT(0); asm volatile("" : "+v"(V[30]), "+v"(V[31]));
    acc = mfma(V[30], pb[14], acc); acc = mfma(V[31], pb[15], acc);
    STAMP(6);
    acc *= fw;
    {
      u32 tk; v4f o0, o1, o2, o3;
      asm volatile("ds_write_b128 %5, %6\n\t"
                   "s_mov_b64 exec, 1\n\tds_add_rtn_u32 %0, %7, %8\n\ts_mov_b64 exec, -1\n\t"
                   "ds_read_b128 %1, %9\n\tds_read_b128 %2, %9 offset:1024\n\tds_read_b128 %3, %9 offset:2048\n\tds_read_b128 %4, %9 offset:3072\n\t"
                   "s_waitcnt lgkmcnt(0)"
                   : "=&v"(tk), "=&v"(o0), "=&v"(o1), "=&v"(o2), "=&v"(o3)
                   : "v"(a_own), "v"(acc), "v"(a_cnt), "v"(one), "v"(a_all) : "memory");
      if (__builtin_amdgcn_readfirstlane(tk) == 3) {
        const v4f o = (o0 + o1) + (o2 + o3);
        asm volatile("s_mov_b64 exec, 0x0f0f\n\tglobal_store_dwordx4 %0, %1, %2 sc1\n\ts_mov_b64 exec, -1" :: "v"(loff), "v"(lsev), "s"(p.ws) : "memory");
        st16_dev(p.ws, ooff, v4u{pk_bf16(o[0], o[1]), T, pk_bf16(o[2], o[3]), T});
      }
    }
    STAMP(7);
    stamps[11] = stamps[0] + (t_start - lds_word(a_org)) + 200;
    stamps[10] = stamps[0] + (stamps[7] - lds_word(a_org));
    STAMP_FLUSH();
    asm volatile(".Lfd_end:");
    return;
  }

  const int hw = wave - 4;
  __builtin_amdgcn_s_setprio(3);
  u32 ep;
  { const u32 z = 0; asm volatile("global_load_dword %0, %1, %2 sc1" : "=v"(ep) : "v"(z), "s"(p.ws) : "memory"); }
  const int drow = lane >> 3, dch = (lane & 7) ^ ((drow >> 1) & 3);
    {
      const int sl = jc >> 3;
      if (sl < 3 || xc < 2) {
        const char* m_ = sl == 0 ? p.wq : (sl == 2 || (sl == 3 && xc == 1)) ? p.wu : p.wg;
        const u32 rowb = sl == 0 ? 8192 : 9216;
        const u32 un = (sl == 3 ? 64 : 8 * xc) + ((3u - (u32)((uintptr_t)m_ >> 7)) & 7u);
        const u32 o_ = (lane >> 3) * rowb + (lane & 7) * 16;
        const char* b_ = m_ + (u32)(144 * hw + 16 + 8 * (jc & 7)) * rowb + un * 128;
        DMA16(o_, b_, lbase + L_OS + hw * 1024, "");
        DMA16(o_, b_ + 64 * rowb, lbase + L_OS + hw * 1024, "");
      }
    }
  if (hw == 0 && lane < 16) *(LAS u32*)(lds + L_CTL + lane * 4) = lane == 10 ? t_start : 0u;
  if (!isS) {
    const u32 xo = (lane & 7) * 1152 + (lane >> 3) * 16;
#pragma unroll
    for (int i = 0; i < 3; ++i) { const int c = min(hw + 4 * i, 8); DMA16(xo, p.x + c * 128, lbase + L_X + c * 1024, ""); }
  }
  const int beta = 2 * (n >> 2) + (n & 1);
  const int us = xc * 6 + (jc - 26), rg = us / 3;
  {
    const u32 rowb = isS ? 1152 : kind == 0 ? 8192 : 9216;
    const char* src = isS ? p.wd + (size_t)(288 * rg + 144 * (hw & 1)) * 1152 + (3 * (us % 3) + (hw >> 1)) * 128
                          : w1 + (size_t)(144 * hw) * rowb + unit * 128;
    const u32 so = drow * rowb + dch * 16;
    u32 dst = lbase + L_T1 + 18 * hw * 1024;
    if (slow) __builtin_amdgcn_s_sleep(SLOW_SLEEP);
    u32 tq = t_start * 4 + S1_ORG * 4;
    int n1 = 18;
    NOUNROLL for (int i = 0; i < n1; ++i) {
      if (!slow) { NOUNROLL while ((int)(now() * 4 - tq) < 0) __builtin_amdgcn_s_sleep(1); }
      tq += S1_DT;
      DMA16(so, src, dst, " nt"); src += 8 * rowb; dst += 1024; }
    (void)tq;
  }
  asm volatile("s_waitcnt lgkmcnt(0)" ::: "memory");
  __builtin_amdgcn_s_barrier();
  asm volatile("" ::: "memory");
  STAMP(1);
  VMCNT(18);
  u32 T;
  asm volatile("v_readfirstlane_b32 %0, %1\n\ts_nop 1" : "=s"(T) : "v"(ep) : "memory");
  T += 1u;
  lds_inc(lbase + C_X);
  if (bid < 8 && hw == 3 && lane == 0) st8_dev(p.ws, WS_ST + bid * 8, v2u{t_start, T});
  v4u xf[18];
  if (!isS) {
    lds_wait_ge<1>(lbase + C_X, 4);
    u32 xa = L_X + (g >> 1) * 128 + beta * 16 + (g & 1) * 8;
    asm volatile("" : "+v"(xa));
#pragma unroll
    for (int ks = 0; ks < 18; ++ks) {
      const v2u x0 = *(LAS v2u*)(lds + xa + ks * 512), x1 = *(LAS v2u*)(lds + xa + ks * 512 + 256);
      xf[ks] = v4u{x0.x, x0.y, x1.x, x1.y};
    }
  }
  VMCNT(0);
  lds_inc(lbase + C_S1);
  if (!isS) {
    u32 ta = L_T1 + tr_lane_addr(lane, hw), tb2 = ta + 9 * 4096;
    asm volatile("" : "+v"(ta), "+v"(tb2));
    lds_wait_ge<1>(lbase + C_S1, 4);
    STAMP(2);
    v4u bf[18];
#pragma unroll
    for (int ks = 0; ks < 18; ++ks) bf[ks] = ks < 9 ? trfrag(lds, ta, ks * 4096) : trfrag(lds, tb2, (ks - 9) * 4096);
    __builtin_amdgcn_sched_barrier(0);
    v4f acc = {0.f, 0.f, 0.f, 0.f}, acc2 = {0.f, 0.f, 0.f, 0.f};
#pragma unroll
    for (int ks = 0; ks < 18; ks += 2) { acc = mfma(xf[ks], bf[ks], acc); acc2 = mfma(xf[ks + 1], bf[ks + 1], acc2); }
    acc += acc2;
    float a0 = acc[0], a1 = acc[1];
    if (kind == 1) {
      a0 = bf16r(a0); a1 = bf16r(a1);
      a0 = a0 * rcp(1.f + __expf(-a0)); a1 = a1 * rcp(1.f + __expf(-a1));
    }
    const float n0 = dpp<XOR1>(a0), n1 = dpp<XOR1>(a1);
    const u32 pk = (n & 1) ? pk_bf16(n1, a1) : pk_bf16(a0, n0);
    const u32 pk2 = __builtin_bit_cast(u32, dpp<XOR2>(__builtin_bit_cast(float, pk)));
    const u32 ao = WS_A1 + (kind == 0 ? 0 : kind == 1 ? A1_SG : A1_UP) + (unit * 4 + hw) * 512 + (2 * g + (n & 1)) * 64 + (n >> 2) * 16;
    if ((n & 2) == 0) st16_dev(p.ws, ao, v4u{pk, T, pk2, T});
    STAMP(3);
  }

  if (hw == 2 && bid == 0) {
    const v4u* gp = (const v4u*)p.gw + (lane & 7);
    v4u gv[9];
#pragma unroll
    for (int i = 0; i < 9; ++i) gv[i] = gp[8 * i];
    float sum = 0.f;
#pragma unroll
    for (int i = 0; i < 9; ++i) {
      const v4u a = *(LAS v4u*)(lds + L_X + (lane & 7) * 128 + (lane >> 3) * 16 + i * 1024);
#pragma unroll
      for (int e = 0; e < 4; ++e) { sum = fmaf(lo_f(a[e]), lo_f(gv[i][e]), sum); sum = fmaf(hi_f(a[e]), hi_f(gv[i][e]), sum); }
    }
    sum += dpp<XOR1>(sum); sum += dpp<XOR2>(sum); sum += dpp<HMIR>(sum);
    const float gt = rcp(1.f + __expf(-sum));
    if ((lane & 7) == 0) st8_dev(p.ws, WS_GATE + (lane >> 3) * 8, v2u{__float_as_uint(gt), T});
  }
  const int ntask = isS ? 0 : jc < 10 ? 2 : 1;
  if (hw == 3) {
    if (isS) lds_wait_ge<1>(lbase + C_S1, 4);
    {
      const u32 so_ = WS_ST + (lane & 3) * 16;
      v4u sv;
      wait_until<4>(t_start + ST_AT);
      asm volatile("global_load_dwordx4 %0, %1, %2 sc1\n\ts_waitcnt vmcnt(0)" : "=&v"(sv) : "v"(so_), "s"(p.ws) : "memory");
      float dsum = (float)(int)(sv.x - t_start) + (float)(int)(sv.z - t_start);
      dsum += dpp<XOR1>(dsum); dsum += dpp<XOR2>(dsum);
      if (ballot_nz(tagbad(0u, sv, T)) == 0) *(LAS u32*)(lds + C_ORG) = t_start + (u32)(int)fminf(fmaxf(dsum * 0.125f, -200.f), 200.f);
    }
    {
      const u32 qo = WS_A1 + (16 * hk + (lane >> 2)) * 512 + bat * 64 + (lane & 3) * 16, go = WS_GATE + (lane & 3) * 16;
      v4u qv, gg;
      wait_until<4>(t_start + Q_AT);
      NOUNROLL for (u32 spins = 0; spins < SPIN_CAP; ++spins) {
        asm volatile("global_load_dwordx4 %0, %2, %4 sc1\n\tglobal_load_dwordx4 %1, %3, %4 sc1\n\ts_waitcnt vmcnt(0)" : "=&v"(qv), "=&v"(gg) : "v"(qo), "v"(go), "s"(p.ws) : "memory");
        if (ballot_nz(tagbad(tagbad(0u, qv, T), gg, T)) == 0) break;
        __builtin_amdgcn_s_sleep(8);
      }
      *(LAS v2u*)(lds + L_Q + (lane >> 4) * 128 + ((lane >> 1) & 1) * 64 + ((lane >> 2) & 3) * 16 + (lane & 1) * 8) = v2u{qv.x, qv.z};
      *(LAS v2u*)(lds + L_W + 128 + (lane & 3) * 8) = v2u{gg.x, gg.z};
      *(LAS u32*)(lds + C_T) = T;
      lds_inc(lbase + C_Q);
    }
    STAMP(4);
    STAMP_FLUSH();
    return;
  }

  const u32 pst = n * 64 + g * 16;
  if (isS) {
    const int cg = 3 * (us % 3) + hw;
    const u32 tb = L_T1 + hw * 36864;
    if (hw == 2) {
      const char* src = p.wd + (size_t)(288 * rg) * 1152 + cg * 128;
      const u32 so = drow * 1152 + dch * 16;
      u32 dst = lbase + tb;
      wait_until<8>(t_start + PACE_ORG + 14 * PACE_DT);
      lds_wait_ge<0>(lbase + C_VI, 4);
      STAMP(5);
      u32 tq = lds_word(lbase + C_ORG) + PACE_ORG + 15 * PACE_DT + (3 * PACE_DT) / 4 + S3_DELAY;
      NOUNROLL for (int i = 0; i < 9; ++i) {
        wait_until<0>(tq); tq += S3_GAP;
#pragma unroll
        for (int k = 0; k < 4; ++k) { DMA16(so, src, dst, " nt"); src += 8 * 1152; dst += 1024; }
      }
    } else {
      lds_wait_ge<2>(lbase + C_S1, 4);
      wait_until<8>(t_start + H_AT);
      const int u0 = (9 * rg) >> 1;
      const u32 ho = WS_A1 + A1_SG + u0 * 2048 + hw * 1024 + lane * 16 + 4096, uo = ho + (A1_UP - A1_SG), ho2 = ho + 4096, uo2 = uo + 4096;
      v4u h[10];
      NOUNROLL for (u32 spins = 0; spins < SPIN_CAP; ++spins) {
#define LH(i, a, o) "global_load_dwordx4 %" #i ", %" #a ", %14 offset:" #o " sc1\n\ts_sleep " H_GAP "\n\t"
        asm volatile(LH(0, 10, -4096) LH(1, 10, -2048) LH(2, 10, 0) LH(3, 10, 2048) LH(4, 12, 0)
                     LH(5, 11, -4096) LH(6, 11, -2048) LH(7, 11, 0) LH(8, 11, 2048) LH(9, 13, 0) "s_waitcnt vmcnt(0)"
                     : "=&v"(h[0]), "=&v"(h[1]), "=&v"(h[2]), "=&v"(h[3]), "=&v"(h[4]), "=&v"(h[5]), "=&v"(h[6]), "=&v"(h[7]), "=&v"(h[8]), "=&v"(h[9])
                     : "v"(ho), "v"(uo), "v"(ho2), "v"(uo2), "s"(p.ws) : "memory");
        u32 bad = 0;
#pragma unroll
        for (int i = 0; i < 10; ++i) bad = tagbad(bad, h[i], T);
        if (ballot_nz(bad) == 0) break;
        __builtin_amdgcn_s_sleep(8);
      }
#define HMUL(a, b) pk_bf16(lo_f(a) * lo_f(b), hi_f(a) * hi_f(b))
      const u32 hl = L_HA + ((lane >> 1) & 1) * 512 + (2 * (2 * hw + (lane >> 5)) + (lane & 1)) * 64 + ((lane >> 2) & 7) * 8;
#pragma unroll
      for (int k = 0; k < 5; ++k) *(LAS v2u*)(lds + hl + k * 1024) = v2u{HMUL(h[k].x, h[5 + k].x), HMUL(h[k].z, h[5 + k].z)};
      lds_inc(lbase + C_H);
    }
    STAMP(6);
    lds_wait_ge<1>(lbase + C_H, 2);
    v4u A[9];
    {
      u32 ha = L_HA + (rg & 1) * 512 + g * 64 + beta * 8;
      asm volatile("" : "+v"(ha));
#pragma unroll
      for (int ks = 0; ks < 9; ++ks) {
        const v2u a0 = *(LAS v2u*)(lds + ha + ks * 512), a1 = *(LAS v2u*)(lds + ha + ks * 512 + 256);
        A[ks] = v4u{a0.x, a0.y, a1.x, a1.y};
      }
    }
    u32 ta[4];
    v4f acc[4];
#pragma unroll
    for (int j = 0; j < 4; ++j) { ta[j] = tb + tr_lane_addr(lane, j); acc[j] = v4f{0.f, 0.f, 0.f, 0.f}; }
    v4u Bc[4], Bn[4];
#define DREAD(D, ks) _Pragma("unroll") for (int j = 0; j < 4; ++j) D[j] = trfrag(lds, ta[j], (ks) * 4096)
#define DMMA(D, ks) _Pragma("unroll") for (int j = 0; j < 4; ++j) acc[j] = mfma(A[ks], D[j], acc[j])
#define DSTEP(CUR, NXT, ks, N) do { VMCNT(N); DREAD(NXT, (ks) + 1); __builtin_amdgcn_sched_barrier(0); DMMA(CUR, ks); __builtin_amdgcn_sched_barrier(0); } while (0)
    VMCNT(32); DREAD(Bc, 0);
    DSTEP(Bc, Bn, 0, 28); DSTEP(Bn, Bc, 1, 24); DSTEP(Bc, Bn, 2, 20); DSTEP(Bn, Bc, 3, 16); DSTEP(Bc, Bn, 4, 12); DSTEP(Bn, Bc, 5, 8); DSTEP(Bc, Bn, 6, 4); DSTEP(Bn, Bc, 7, 0);
    DMMA(Bc, 8);
    const u32 po = WS_P + (32 + rg) * P_ROW + cg * 2048 + pst;
    if (PD_AT) { asm volatile("" : "+v"(acc[0]), "+v"(acc[1]), "+v"(acc[2]), "+v"(acc[3])); wait_until<4>(lds_word(lbase + C_ORG) + PACE_ORG + 15 * PACE_DT + (3 * PACE_DT) / 4 + PD_AT); }
    st16_dev(p.ws, po, v4u{pk_bf16(acc[0][0], acc[0][1]), T, pk_bf16(acc[1][0], acc[1][1]), T});
    st16_dev(p.ws, po + 1024, v4u{pk_bf16(acc[2][0], acc[2][1]), T, pk_bf16(acc[3][0], acc[3][1]), T});
    STAMP(8);
    STAMP_FLUSH();
    return;
  }
  if (hw < ntask) {
    const int task = hw == 0 ? jc : 26 + jc, head = 4 * xc + task / 9, cg = task % 9, tau = xc * 36 + task;
    const u32 tb = L_WO + hw * 16384;
    {
      const char* src = p.wo + (size_t)(128 * head) * 1152 + cg * 128;
      const u32 so = (8 * (drow & 3) + (drow >> 2)) * 1152 + dch * 16;
      u32 dst = lbase + tb;
      wait_until<8>(t_start + PACE_ORG + 14 * PACE_DT);
      lds_wait_ge<0>(lbase + C_VI, 4);
      STAMP(4);
      u32 tq = lds_word(lbase + C_ORG) + PACE_ORG + 15 * PACE_DT + (3 * PACE_DT) / 4 + S2_DELAY;
      NOUNROLL for (int i = 0; i < 4; ++i) {
        wait_until<0>(tq); tq += S2_GAP;
#pragma unroll
        for (int k = 0; k < 4; ++k) { DMA16(so, src + 2 * k * 1152, dst, " nt"); dst += 1024; }
        src += 32 * 1152;
      }
    }
    u32 oo = WS_O + head * 8192 + n * 64 + g * 16, oo2 = oo + 4096;
#define LO(i, a, o) "global_load_dwordx4 %" #i ", %" #a ", %12 offset:" #o "*1024 sc1\n\t"
    u32 po = WS_P + head * P_ROW + cg * 2048 + pst;
    u32 ro0 = WS_P + (lane >> 2) * P_ROW + tau * 64 + (lane & 3) * 16, ro1 = ro0 + 16 * P_ROW, ro2 = ro0 + 32 * P_ROW;
    const int tl = tau & 31;
    u32 outo = ((2 * (lane & 3) + ((lane >> 2) & 1)) * 576 + (tau >> 5) * 64 + ((tl >> 3) & 1) * 32 + (tl >> 4) * 16 + (tl & 7) + ((lane >> 3) & 1) * 8) * 2;
    u32 ta[4];
#pragma unroll
    for (int j = 0; j < 4; ++j) ta[j] = tb + tr_lane_addr(lane, j);
    asm volatile("" : "+v"(oo), "+v"(oo2), "+v"(po), "+v"(ro0), "+v"(ro1), "+v"(ro2), "+v"(outo), "+v"(ta[0]), "+v"(ta[1]), "+v"(ta[2]), "+v"(ta[3]));
    v2f gv = *(LAS v2f*)(lds + L_W + 128 + (lane & 3) * 8);
    u32 lo = WS_LSE + (head * 16 + 4 * g + (n & 3)) * 64;
    asm volatile("" : "+v"(gv), "+v"(lo));
    const u32 xs = lds_word(lbase + C_ORG) + PACE_ORG + 15 * PACE_DT + (3 * PACE_DT) / 4 + M1;
    lds_wait_ge<0>(lbase + C_O, 4);
    u32 x1 = now() + M1_OWN;
    if ((int)(x1 - xs) < 0) x1 = xs;
    u32 tries = 0;
    {
      v4u u[8], B[16];
      v2u e;
#define SWEEP_O(C, PRE, POST) asm volatile(PRE "global_load_dwordx2 %8, %11, %12 sc1\n\t" LO(0, 9, 0) LO(1, 9, 1) LO(2, 9, 2) LO(3, 9, 3) LO(4, 10, 0) LO(5, 10, 1) LO(6, 10, 2) LO(7, 10, 3) POST \
                     : C(u[0]), C(u[1]), C(u[2]), C(u[3]), C(u[4]), C(u[5]), C(u[6]), C(u[7]), C(e) \
                     : "v"(oo), "v"(oo2), "v"(lo), "s"(p.ws), "s"(need) : "memory")
      u64 need = ~0ull;
      wait_until<0>(x1);
      STAMP(5);
      SWEEP_O("=&v", "", "s_waitcnt vmcnt(9)");
      STAMP(9);
#pragma unroll
      for (int j = 0; j < 4; ++j)
#pragma unroll
        for (int ks = 0; ks < 4; ++ks) B[4 * j + ks] = trfrag(lds, ta[j], ks * 4096);
      asm volatile("s_waitcnt vmcnt(0)" : "+v"(u[0]), "+v"(u[1]), "+v"(u[2]), "+v"(u[3]), "+v"(u[4]), "+v"(u[5]), "+v"(u[6]), "+v"(u[7]), "+v"(e) :: "memory");
      NOUNROLL for (u32 spins = 0; spins < SPIN_CAP; ++spins) {
        u32 bad = e.y ^ T;
#pragma unroll
        for (int i = 0; i < 8; ++i) bad = tagbad(bad, u[i], T);
        need = ballot_nz(bad);
        if (need == 0) break;
        tries += 10;
        SWEEP_O("+v", "s_sleep 2\n\ts_mov_b64 exec, %13\n\t", "s_mov_b64 exec, -1\n\ts_waitcnt vmcnt(0)");
      }
      STAMP(6);
      const float me = __uint_as_float(e.x), wme = rcp(1.f + __builtin_amdgcn_exp2f(dpp<XOR1>(me) - me));
      const v4f wv = {dpp<0x00>(wme), dpp<0x55>(wme), dpp<0xAA>(wme), dpp<0xFF>(wme)};
      v4f acc[4];
#pragma unroll
      for (int j = 0; j < 4; ++j) acc[j] = v4f{0.f, 0.f, 0.f, 0.f};
#pragma unroll
      for (int ks = 0; ks < 4; ++ks) {
        const v4u A = {u[2 * ks].x, u[2 * ks].z, u[2 * ks + 1].x, u[2 * ks + 1].z};
#pragma unroll
        for (int j = 0; j < 4; ++j) acc[j] = mfma(A, B[4 * j + ks], acc[j]);
      }
#define MIX(j) pk_bf16(fmaf(wv[0], acc[j][0], wv[1] * acc[j][1]), fmaf(wv[2], acc[j][2], wv[3] * acc[j][3]))
      st16_dev(p.ws, po, v4u{MIX(0), T, MIX(1), T});
      st16_dev(p.ws, po + 1024, v4u{MIX(2), T, MIX(3), T});
    }
    STAMP(7);
    {
      v4u a[3];
      u64 nd[3];
#define LP(i, o, m) "s_mov_b64 exec, %" #m "\n\tglobal_load_dwordx4 %" #i ", %" #o ", %6 sc1\n\t"
#pragma unroll
      for (int i = 0; i < 3; ++i) { a[i] = v4u{0u, 0u, 0u, 0u}; nd[i] = ~0ull; }
      wait_until<0>(x1 + D2);
      STAMP(10);
      NOUNROLL for (u32 spins = 0; spins < SPIN_CAP; ++spins) {
        asm volatile(LP(0, 3, 7) LP(1, 4, 8) LP(2, 5, 9) "s_mov_b64 exec, -1\n\ts_waitcnt vmcnt(0)"
                     : "+v"(a[0]), "+v"(a[1]), "+v"(a[2]) : "v"(ro0), "v"(ro1), "v"(ro2), "s"(p.ws), "s"(nd[0]), "s"(nd[1]), "s"(nd[2]) : "memory");
#pragma unroll
        for (int i = 0; i < 3; ++i) nd[i] = ballot_nz(tagbad(0u, a[i], T));
        if ((nd[0] | nd[1] | nd[2]) == 0) break;
        tries += 1;
        __builtin_amdgcn_s_sleep(2);
      }
      const float v00 = gv[0] * (lo_f(a[0].x) + lo_f(a[1].x)) + (1.f - gv[0]) * lo_f(a[2].x), v01 = gv[1] * (hi_f(a[0].x) + hi_f(a[1].x)) + (1.f - gv[1]) * hi_f(a[2].x);
      const float v10 = gv[0] * (lo_f(a[0].z) + lo_f(a[1].z)) + (1.f - gv[0]) * lo_f(a[2].z), v11 = gv[1] * (hi_f(a[0].z) + hi_f(a[1].z)) + (1.f - gv[1]) * hi_f(a[2].z);
      const bool b2 = (lane & 4) != 0, b3 = (lane & 8) != 0;
      const float c0 = (b2 ? v01 : v00) + dpp<ROR4>(b2 ? v00 : v01), c1 = (b2 ? v11 : v10) + dpp<ROR4>(b2 ? v10 : v11);
      const float r = xg_sum((b3 ? c1 : c0) + dpp<ROR8>(b3 ? c0 : c1));
      if (lane < 16) *(unsigned short*)(p.out + outo) = (unsigned short)pk_bf16(r, r);
      if (tau == 0 && lane == 0) __hip_atomic_store((u32*)(p.ws + WS_EPOCH), T, __ATOMIC_RELAXED, __HIP_MEMORY_SCOPE_AGENT);
    }
    STAMP(8);
    stamps[11] = stamps[0] + (stamps[8] - lds_word(lbase + C_ORG));
    (void)tries;
  }
  STAMP_FLUSH();
}

extern "C" void kernel_launch(void* const* d_in, const int* in_sizes, int n_in, void* d_out, int out_size, void* d_ws, size_t ws_size,
                              hipStream_t stream) {
  Params p;
  p.x = (const char*)d_in[0]; p.k = (const char*)d_in[1]; p.v = (const char*)d_in[2]; p.wq = (const char*)d_in[3]; p.wo = (const char*)d_in[4];
  p.wg = (const char*)d_in[6]; p.wu = (const char*)d_in[7]; p.wd = (const char*)d_in[8]; p.gw = (const char*)d_in[9];
  p.out = (char*)d_out; p.ws = (char*)d_ws;
  hipLaunchKernelGGL(fused_decode, dim3(256), dim3(512), 0, stream, p);
}
```

```cpp
#define M2 300 M1=210
#define PROBE_ST 2
#define PROBE_COND (wave>=4)
#include <hip/hip_runtime.h>
#include <stdint.h>

typedef unsigned u32;
typedef unsigned long long u64;
typedef u32 v4u __attribute__((ext_vector_type(4)));
typedef u32 v2u __attribute__((ext_vector_type(2)));
typedef float v4f __attribute__((ext_vector_type(4)));
typedef float v2f __attribute__((ext_vector_type(2)));
typedef short v4s __attribute__((ext_vector_type(4)));
typedef __bf16 v8bf __attribute__((ext_vector_type(8)));
typedef __bf16 v2bf __attribute__((ext_vector_type(2)));
#define LAS __attribute__((address_space(3)))
#define DEVINL __device__ __forceinline__
#define NOUNROLL _Pragma("nounroll")

#define S1_ORG 80
#define S1_DT 57
#define PACE_ORG 340
#define PACE_DT 58
#define ST_AT 470
#define Q_AT 600
#define H_AT 700
#define H_GAP "6"
#define S2_DELAY 50
#define S2_GAP 18
#define M1 145
#define M1_OWN 20
#define M2_OWN 10
#define SKS 6
constexpr u32 T_LAST = PACE_ORG + 14 * PACE_DT + (3 * PACE_DT) / 4;

constexpr u32 WS_EPOCH = 0;
constexpr u32 WS_GATE = 256;
constexpr u32 WS_ST = 512;
constexpr u32 WS_A1 = 4096;
constexpr u32 A1_SG = 64 * 2048, A1_UP = (64 + 72) * 2048;
constexpr u32 WS_O = WS_A1 + 208 * 2048;
constexpr u32 O_HEAD = 9 * 1024;
constexpr u32 WS_P = WS_O + 32 * O_HEAD;
constexpr u32 P_ROW = 288 * 64;
constexpr u32 WS_DIAG = 4u << 20;
constexpr u32 L_T1 = 0;
constexpr u32 L_X = 73728;
constexpr u32 L_WO = L_X + 9216;
constexpr u32 L_HA = 110592;
constexpr u32 L_Q = 115712;
constexpr u32 L_ML = L_Q + 512;
constexpr u32 L_CTL = L_ML + 64;
constexpr u32 L_G = L_CTL + 64;
constexpr u32 L_OS = L_G + 128;
constexpr u32 L_SCR = L_OS + 4096;
constexpr u32 L_END = L_SCR + 1024;
constexpr u32 C_S1 = L_CTL + 0, C_Q = L_CTL + 4, C_ML = L_CTL + 8, C_O = L_CTL + 12, C_VI = L_CTL + 16, C_H = L_CTL + 20, C_X = L_CTL + 24,
              C_D3 = L_CTL + 28, C_T = L_CTL + 32, C_ORG = L_CTL + 40;
constexpr u32 SPIN_CAP = 1u << 17;

struct Params {
  const char* k; const char* v; const char* x; const char* wq; const char* wo; const char* wg; const char* wu; const char* wd;
  const char* gw; char* out; char* ws;
};

DEVINL v4f mfma(v4u a, v4u b, v4f c) {
  return __builtin_amdgcn_mfma_f32_16x16x32_bf16(__builtin_bit_cast(v8bf, a), __builtin_bit_cast(v8bf, b), c, 0, 0, 0);
}
DEVINL u32 pk_bf16(float lo, float hi) { v2f f = {lo, hi}; return __builtin_bit_cast(u32, __builtin_convertvector(f, v2bf)); }
DEVINL float bf16r(float f) { return __builtin_bit_cast(float, pk_bf16(f, f) & 0xffff0000u); }
DEVINL float lo_f(u32 v) { return __uint_as_float(v << 16); }
DEVINL float hi_f(u32 v) { return __uint_as_float(v & 0xffff0000u); }
DEVINL float dot2(u32 a, u32 b, float c) { return __builtin_amdgcn_fdot2_f32_bf16(__builtin_bit_cast(v2bf, a), __builtin_bit_cast(v2bf, b), c, false); }
DEVINL float rcp(float x) { return __builtin_amdgcn_rcpf(x); }
template <int CTRL> DEVINL float dpp(float x) {
  return __builtin_bit_cast(float, __builtin_amdgcn_mov_dpp(__builtin_bit_cast(int, x), CTRL, 0xf, 0xf, true));
}
constexpr int XOR1 = 0xB1, XOR2 = 0x4E, ROR4 = 0x124, ROR8 = 0x128, HMIR = 0x141;
DEVINL float xg_max(float x) {
  auto s = __builtin_amdgcn_permlane16_swap(__float_as_uint(x), __float_as_uint(x), false, false);
  x = fmaxf(__uint_as_float(s[0]), __uint_as_float(s[1]));
  auto t = __builtin_amdgcn_permlane32_swap(__float_as_uint(x), __float_as_uint(x), false, false);
  return fmaxf(__uint_as_float(t[0]), __uint_as_float(t[1]));
}
DEVINL float xg_sum(float x) {
  auto s = __builtin_amdgcn_permlane16_swap(__float_as_uint(x), __float_as_uint(x), false, false);
  x = __uint_as_float(s[0]) + __uint_as_float(s[1]);
  auto t = __builtin_amdgcn_permlane32_swap(__float_as_uint(x), __float_as_uint(x), false, false);
  return __uint_as_float(t[0]) + __uint_as_float(t[1]);
}
DEVINL u32 tagbad(u32 acc, v4u u, u32 T) {
  acc = __builtin_amdgcn_bitop3_b32(acc, u.y, T, 0xF6);
  return __builtin_amdgcn_bitop3_b32(acc, u.w, T, 0xF6);
}
DEVINL u64 ballot_nz(u32 v) { return __builtin_amdgcn_ballot_w64(v != 0); }
DEVINL void lds_inc(u32 addr) {
  const u32 one = 1;
  asm volatile("s_mov_b64 exec, 1\n\tds_add_u32 %0, %1\n\ts_mov_b64 exec, -1" :: "v"(addr), "v"(one) : "memory");
}
DEVINL u32 lds_word(u32 addr) {
  u32 r;
  asm volatile("ds_read_b32 %0, %1\n\ts_waitcnt lgkmcnt(0)" : "=v"(r) : "v"(addr) : "memory");
  return __builtin_amdgcn_readfirstlane(r);
}
template <int SLEEP> DEVINL void lds_wait_ge(u32 addr, u32 want) {
  NOUNROLL for (u32 spins = 0; spins < (SPIN_CAP << 5); ++spins) {
    if (lds_word(addr) >= want) break;
    if (SLEEP) __builtin_amdgcn_s_sleep(SLEEP);
  }
}
DEVINL u32 now() { return (u32)__builtin_amdgcn_s_memrealtime(); }
template <int SLEEP> DEVINL void wait_until(u32 t) {
  NOUNROLL while ((int)(now() - t) < 0) { if (SLEEP) __builtin_amdgcn_s_sleep(SLEEP); }
}
#define DMA16(voff, sbase, ldsdst, POL) \
  asm volatile("s_mov_b32 m0, %2\n\ts_nop 0\n\tglobal_load_lds_dwordx4 %0, %1" POL :: "v"(voff), "s"(sbase), "s"(ldsdst) : "memory")
#define VMCNT_(n) asm volatile("s_waitcnt vmcnt(" #n ")" ::: "memory")
#define VMCNT(n) VMCNT_(n)
DEVINL void st16_dev(char* base, u32 off, v4u d) { asm volatile("global_store_dwordx4 %0, %1, %2 sc1\n\ts_nop 1" :: "v"(off), "v"(d), "s"(base) : "memory"); }
DEVINL void st8_dev(char* base, u32 off, v2u d) { asm volatile("global_store_dwordx2 %0, %1, %2 sc1" :: "v"(off), "v"(d), "s"(base) : "memory"); }
DEVINL v4u trfrag(LAS char* l, u32 a0, u32 imm) {
  const v2u x = __builtin_bit_cast(v2u, __builtin_amdgcn_ds_read_tr16_b64_v4i16((LAS v4s*)(l + a0 + imm)));
  const v2u y = __builtin_bit_cast(v2u, __builtin_amdgcn_ds_read_tr16_b64_v4i16((LAS v4s*)(l + a0 + imm + 2048)));
  return v4u{x.x, x.y, y.x, y.y};
}
DEVINL u32 tr_lane_addr(int lane, int j) {
  const int r0 = (lane >> 2) & 15, p = lane & 3;
  const int c = (p < 2 ? j : j + 4) ^ ((r0 >> 1) & 3);
  return r0 * 128 + c * 16 + 8 * (p & 1);
}

#define STAMP(i) do { stamps[i] = now(); } while (0)
#define STAMP_DECL u32 stamps[12] = {0, 0, 0, 0, 0, 0, 0, 0, 0, 0, 0, 0}
#define STAMP_FLUSH() do { u32 tgt_ = 4000; const u32 o_ = lds_word(lbase + C_ORG); \
  if (PROBE_ST >= 0 && (PROBE_COND) && stamps[PROBE_ST < 0 ? 0 : PROBE_ST]) tgt_ += 10 * (stamps[PROBE_ST < 0 ? 0 : PROBE_ST] - o_); \
  wait_until<1>(t_start + 3000); wait_until<1>(lds_word(lbase + C_ORG) + tgt_); } while (0)

__global__ void __launch_bounds__(512, 2) fused_decode(Params p) {
  __shared__ __attribute__((aligned(1024))) char lds_raw[L_END];
  LAS char* lds = (LAS char*)lds_raw;
  const u32 lbase = (u32)(uintptr_t)lds;
  const int tid = threadIdx.x, lane = tid & 63, wave = __builtin_amdgcn_readfirstlane(tid >> 6);
  const int bid = blockIdx.x, xc = bid & 7, jc = bid >> 3;
  const int hk = 2 * xc + (jc & 1), half = (jc >> 1) & 1, bat = jc >> 2;
  const int g = lane >> 4, n = lane & 15;
  STAMP_DECL;
  const u64 t_start64 = __builtin_amdgcn_s_memrealtime();
  u32 rot = bid; rot *= 0x9E3779B1u; rot ^= rot >> 15; rot *= 0x85EBCA77u; rot >>= 13;
  u32 reg = ((u32)(bat * 16 + hk) * 4096 + 3072 + half * 512 + ((wave + (rot >> 3)) & 3) * 16) * 256;
  rot &= 7;
  constexpr u32 TSTR = 16384;
  u32 reg0 = reg + rot * TSTR;
  u32 koff = (n >> 2) * 256 + g * 64 + (n & 3) * 16;
  asm volatile("" : "+s"(reg), "+s"(rot), "+s"(reg0));
  asm volatile("" : "+v"(koff));
  {
    const char* pc; u32 len;
    asm volatile("s_getpc_b64 %0\n.Lfd_anchor:\n\ts_mov_b32 %1, .Lfd_end-.Lfd_anchor" : "=s"(pc), "=s"(len));
    const u32 po_ = (u32)(jc * 8 + wave) * 1024u;
    if (po_ < len) { const u32 lo_ = lane * 16; DMA16(lo_, (const char*)((uintptr_t)(pc + min(po_, len - 1024u)) & ~(uintptr_t)15), lbase + L_SCR, ""); }
  }
  asm volatile("" :: "s"(p.x), "s"(p.k), "s"(p.v), "s"(p.wq), "s"(p.wo), "s"(p.wg), "s"(p.wu), "s"(p.wd), "s"(p.gw), "s"(p.out), "s"(p.ws), "s"(t_start64));
  const u32 t_start = (u32)t_start64;
  stamps[0] = t_start;

  if (__builtin_expect(wave < 4, 1)) {
    const char* kb = p.k + reg;
    const char* vb = p.v + reg;
    const u32 voff = lane * 16;
    v4u K[32], V[32];
#define LD4(A, b_, off, t) do { \
    asm volatile("global_load_dwordx4 %0, %4, %5 nt\n\tglobal_load_dwordx4 %1, %4, %5 offset:1024 nt\n\t" \
                 "global_load_dwordx4 %2, %4, %5 offset:2048 nt\n\tglobal_load_dwordx4 %3, %4, %5 offset:3072 nt" \
                 : "=&v"(A[4 * (t)]), "=&v"(A[4 * (t) + 1]), "=&v"(A[4 * (t) + 2]), "=&v"(A[4 * (t) + 3]) : "v"(off), "s"(b_) : "memory"); } while (0)
#define LDK(t) LD4(K, kb + (((t) + rot) & 7) * TSTR, koff, t)
#define LDV(t) LD4(V, vb + (((t) + rot) & 7) * TSTR, voff, t)
#define TIE4(A, t) asm volatile("" : "+v"(A[4 * (t)]), "+v"(A[4 * (t) + 1]), "+v"(A[4 * (t) + 2]), "+v"(A[4 * (t) + 3]))
    LD4(K, p.k + reg0, koff, 0);
    STAMP(1);
    if (tid < 16) *(LAS u32*)(lds + L_CTL + lane * 4) = lane == 10 ? t_start : 0u;
    asm volatile("s_waitcnt lgkmcnt(0)" ::: "memory");
    __builtin_amdgcn_s_barrier();
    const u32 t_off = PACE_ORG + (wave * PACE_DT) / 4, a_org = lbase + C_ORG;
#define PACE(m) wait_until<1>(lds_word(a_org) + t_off + (m) * PACE_DT)
    PACE(0); LDK(1); PACE(1); LDK(2); PACE(2); LDK(3); PACE(3); LDK(4); PACE(4); LDK(5); PACE(5); LDK(6); PACE(6); LDK(7);
    PACE(7); LDV(0); PACE(8); LDV(1); PACE(9); LDV(2);
    STAMP(2);
    lds_wait_ge<1>(lbase + C_Q, 1);
    STAMP(3);
    const v4u qf = *(LAS v4u*)(lds + L_Q + (n >> 3) * 256 + (4 * g + (n & 3)) * 16);
    const u32 T = *(LAS u32*)(lds + C_T);
    float s[32];
    const v4f zero = {0.f, 0.f, 0.f, 0.f};
#define QK(i) do { const v4f d_ = mfma(K[i], qf, zero); s[i] = ((dpp<0x00>(d_[0]) + dpp<0x55>(d_[1])) + dpp<0xAA>(d_[2])) + dpp<0xFF>(d_[3]); } while (0)
    VMCNT(12);
    TIE4(K, 0); TIE4(K, 1); TIE4(K, 2); TIE4(K, 3);
#pragma unroll
    for (int i = 0; i < 16; ++i) QK(i);
    TIE4(s, 0); TIE4(s, 1); TIE4(s, 2); TIE4(s, 3);
    PACE(10); LDV(3);
    TIE4(K, 4); TIE4(K, 5); TIE4(K, 6); TIE4(K, 7);
#pragma unroll
    for (int i = 16; i < 32; ++i) QK(i);
    constexpr float C2 = 0.08838834764831845f * 1.4426950408889634f;
    float mx = s[0];
#pragma unroll
    for (int i = 1; i < 32; ++i) mx = fmaxf(mx, s[i]);
    mx = xg_max(mx) * C2;
    asm volatile("" : "+v"(mx));
    PACE(11); LDV(4);
    asm volatile("" : "+v"(mx));
    float ls = 0.f;
#pragma unroll
    for (int i = 0; i < 32; ++i) { s[i] = __builtin_amdgcn_exp2f(fmaf(s[i], C2, -mx)); ls += s[i]; }
    ls = xg_sum(ls);
    if ((lane & 0x37) == 0) *(LAS v2f*)(lds + L_ML + wave * 16 + (lane >> 3) * 8) = v2f{mx, ls};
    lds_inc(lbase + C_ML);
    PACE(12); LDV(5);
    u32 msk[4];
#pragma unroll
    for (int i = 0; i < 4; ++i) msk[i] = (((lane & 7) >> 1) == i) ? ((lane & 1) ? 0xffff0000u : 0x0000ffffu) : 0u;
    v4f acc = zero;
    VMCNT(8);
    TIE4(V, 0); TIE4(V, 1); TIE4(V, 2); TIE4(V, 3);
#pragma unroll
    for (int i = 0; i < 16; ++i) {
      const u32 pp = pk_bf16(s[i], s[i]);
      acc = mfma(V[i], v4u{pp & msk[0], pp & msk[1], pp & msk[2], pp & msk[3]}, acc);
    }
    asm volatile("" : "+v"(acc));
    PACE(13); LDV(6);
    TIE4(s, 4); TIE4(s, 5); TIE4(s, 6); TIE4(s, 7);
    v4u pb[16];
#pragma unroll
    for (int i = 0; i < 16; ++i) {
      const u32 pp = pk_bf16(s[16 + i], s[16 + i]);
      pb[i] = v4u{pp & msk[0], pp & msk[1], pp & msk[2], pp & msk[3]};
    }
    TIE4(pb, 0); TIE4(pb, 1); TIE4(pb, 2); TIE4(pb, 3);
    PACE(14); LDV(7);
    lds_inc(lbase + C_VI);
    __builtin_amdgcn_s_setprio(3);
    STAMP(4);
    lds_wait_ge<1>(lbase + C_ML, 4);
    float fw;
    u32 lse;
    {
      v2f ml[4];
#pragma unroll
      for (int w = 0; w < 4; ++w) ml[w] = *(LAS v2f*)(lds + L_ML + w * 16 + (n >> 3) * 8);
      const float M = fmaxf(fmaxf(ml[0].x, ml[1].x), fmaxf(ml[2].x, ml[3].x));
      float L = 0.f;
#pragma unroll
      for (int w = 0; w < 4; ++w) L = fmaf(ml[w].y, __builtin_amdgcn_exp2f(ml[w].x - M), L);
      fw = __builtin_amdgcn_exp2f(mx - M) * rcp(L);
      lse = __float_as_uint(M + __builtin_amdgcn_logf(L));
    }
    u32 a_own = lbase + L_OS + wave * 1024 + lane * 16, a_all = lbase + L_OS + lane * 16, a_cnt = lbase + C_O, one = 1;
    u32 ooff = WS_O + (2 * hk + (n >> 3)) * O_HEAD + (2 * g + ((n >> 2) & 1)) * 1024 + (2 * bat + half) * 64 + (n & 3) * 16;
    u32 loff = WS_O + (2 * hk + (n >> 3)) * O_HEAD + 8192 + (2 * bat + half) * 64 + (n & 3) * 16;
    v4u lsev = {lse, T, lse, T};
    asm volatile("" : "+v"(fw), "+v"(acc), "+v"(a_own), "+v"(a_all), "+v"(a_cnt), "+v"(one), "+v"(ooff), "+v"(loff), "+v"(lsev));
    STAMP(5);
    VMCNT(12); TIE4(V, 4);
#pragma unroll
    for (int i = 0; i < 4; ++i) acc = mfma(V[16 + i], pb[i], acc);
    asm volatile("" : "+v"(acc));
    VMCNT(8); TIE4(V, 5);
#pragma unroll
    for (int i = 4; i < 8; ++i) acc = mfma(V[16 + i], pb[i], acc);
    asm volatile("" : "+v"(acc));
    VMCNT(4); TIE4(V, 6);
#pragma unroll
    for (int i = 8; i < 12; ++i) acc = mfma(V[16 + i], pb[i], acc);
    asm volatile("" : "+v"(acc));
    VMCNT(2); asm volatile("" : "+v"(V[28]), "+v"(V[29]));
    acc = mfma(V[28], pb[12], acc); acc = mfma(V[29], pb[13], acc);
    asm volatile("" : "+v"(acc));
    VMCNT(0); asm volatile("" : "+v"(V[30]), "+v"(V[31]));
    acc = mfma(V[30], pb[14], acc); acc = mfma(V[31], pb[15], acc);
    STAMP(6);
    acc *= fw;
    {
      u32 tk; v4f o0, o1, o2, o3;
      asm volatile("ds_write_b128 %5, %6\n\t"
                   "s_mov_b64 exec, 1\n\tds_add_rtn_u32 %0, %7, %8\n\ts_mov_b64 exec, -1\n\t"
                   "ds_read_b128 %1, %9\n\tds_read_b128 %2, %9 offset:1024\n\tds_read_b128 %3, %9 offset:2048\n\tds_read_b128 %4, %9 offset:3072\n\t"
                   "s_waitcnt lgkmcnt(0)"
                   : "=&v"(tk), "=&v"(o0), "=&v"(o1), "=&v"(o2), "=&v"(o3)
                   : "v"(a_own), "v"(acc), "v"(a_cnt), "v"(one), "v"(a_all) : "memory");
      if (__builtin_amdgcn_readfirstlane(tk) == 3) {
        const v4f o = (o0 + o1) + (o2 + o3);
        v4u ov = {pk_bf16(o[0], o[1]), T, pk_bf16(o[2], o[3]), T};
        asm volatile("" : "+v"(ov));
        asm volatile("s_mov_b64 exec, 0x0f0f\n\tglobal_store_dwordx4 %0, %1, %2 sc1\n\ts_mov_b64 exec, -1" :: "v"(loff), "v"(lsev), "s"(p.ws) : "memory");
        st16_dev(p.ws, ooff, ov);
      }
    }
    STAMP(7);
    STAMP_FLUSH();
    return;
  }

  const int hw = wave - 4;
  __builtin_amdgcn_s_setprio(3);
  u32 ep;
  { const u32 z = 0; asm volatile("global_load_dword %0, %1, %2 sc1" : "=v"(ep) : "v"(z), "s"(p.ws) : "memory"); }
  __builtin_amdgcn_s_barrier();
  const bool isS = jc >= 26;
  const int kind = jc < 8 ? 0 : jc < 17 ? 1 : 2;
  const int unit = kind == 0 ? xc * 8 + jc : xc * 9 + jc - (kind == 1 ? 8 : 17);
  const int us = xc * 6 + (jc - 26), rg = us / 3, cg0 = 3 * (us % 3);
  const int drow = lane >> 3, dch = (lane & 7) ^ ((drow >> 1) & 3);
  const int beta = 2 * (n >> 2) + (n & 1);
  if (!isS) {
    const u32 xo = (lane & 7) * 1152 + (lane >> 3) * 16;
    DMA16(xo, p.x + hw * 128, lbase + L_X + hw * 1024, "");
    DMA16(xo, p.x + (hw + 4) * 128, lbase + L_X + (hw + 4) * 1024, "");
    if (hw == 0) DMA16(xo, p.x + 8 * 128, lbase + L_X + 8 * 1024, "");
  }
  STAMP(1);
  {
    const u32 rowb = isS ? 1152 : kind == 0 ? 8192 : 9216;
    const char* src = isS ? p.wd + (u32)(288 * rg + 8 * SKS * hw) * 1152 + cg0 * 128
                          : (kind == 0 ? p.wq : kind == 1 ? p.wg : p.wu) + (u32)(144 * hw) * rowb + unit * 128;
    u32 so = drow * rowb + dch * 16;
    u32 sa = isS ? 128 : 8 * rowb, sb = isS ? 8 * 1152 - 256 : 8 * rowb, da = isS ? 36864 : 1024, db = isS ? 1024 - 2 * 36864 : 1024;
    u32 dst = lbase + L_T1 + (isS ? SKS : 18) * hw * 1024;
    const u32 dt1 = isS ? (S1_DT * 6) / SKS : S1_DT;
    u32 tq = (t_start + S1_ORG) * 4;
#define S1WAIT() do { NOUNROLL while ((int)(now() * 4 - tq) < 0) __builtin_amdgcn_s_sleep(1); tq += dt1; } while (0)
    NOUNROLL for (int i = 0; i < (isS ? SKS : 6); ++i) {
      S1WAIT(); DMA16(so, src, dst, " nt"); src += sa; dst += da;
      S1WAIT(); DMA16(so, src, dst, " nt"); src += sa; dst += da;
      S1WAIT(); DMA16(so, src, dst, " nt"); src += sb; dst += db;
    }
  }
  VMCNT(18);
  u32 T;
  asm volatile("v_readfirstlane_b32 %0, %1\n\ts_nop 1" : "=s"(T) : "v"(ep) : "memory");
  T += 1u;
  lds_inc(lbase + C_X);
  if (bid < 8 && hw == 3 && lane == 0) st8_dev(p.ws, WS_ST + bid * 8, v2u{t_start, T});
  v4u xf[18];
  if (!isS) {
    lds_wait_ge<1>(lbase + C_X, 4);
    u32 xa = L_X + (g >> 1) * 128 + beta * 16 + (g & 1) * 8;
    asm volatile("" : "+v"(xa));
#pragma unroll
    for (int ks = 0; ks < 18; ++ks) {
      const v2u x0 = *(LAS v2u*)(lds + xa + ks * 512), x1 = *(LAS v2u*)(lds + xa + ks * 512 + 256);
      xf[ks] = v4u{x0.x, x0.y, x1.x, x1.y};
    }
  }
  VMCNT(0);
  lds_inc(lbase + C_S1);
  if (!isS) {
    u32 ta = L_T1 + tr_lane_addr(lane, hw), tb2 = ta + 9 * 4096;
    asm volatile("" : "+v"(ta), "+v"(tb2));
    lds_wait_ge<1>(lbase + C_S1, 4);
    STAMP(2);
    v4u bf[18];
#pragma unroll
    for (int ks = 0; ks < 18; ++ks) bf[ks] = ks < 9 ? trfrag(lds, ta, ks * 4096) : trfrag(lds, tb2, (ks - 9) * 4096);
    __builtin_amdgcn_sched_barrier(0);
    v4f acc = {0.f, 0.f, 0.f, 0.f}, acc2 = {0.f, 0.f, 0.f, 0.f};
#pragma unroll
    for (int ks = 0; ks < 18; ks += 2) { acc = mfma(xf[ks], bf[ks], acc); acc2 = mfma(xf[ks + 1], bf[ks + 1], acc2); }
    acc += acc2;
    float a0 = acc[0], a1 = acc[1];
    if (kind == 1) {
      a0 = bf16r(a0); a1 = bf16r(a1);
      a0 = a0 * rcp(1.f + __expf(-a0)); a1 = a1 * rcp(1.f + __expf(-a1));
    }
    const float n0 = dpp<XOR1>(a0), n1 = dpp<XOR1>(a1);
    const u32 pk = (n & 1) ? pk_bf16(n1, a1) : pk_bf16(a0, n0);
    const int sl_ = 16 * (lane >> 3) + 4 * (lane & 3) + ((lane >> 2) & 1);
    const u32 pa = (u32)__builtin_amdgcn_ds_bpermute(4 * (sl_ & 63), (int)pk), pb_ = (u32)__builtin_amdgcn_ds_bpermute(4 * ((sl_ + 2) & 63), (int)pk);
    const u32 ao = WS_A1 + (kind == 0 ? 0 : kind == 1 ? A1_SG : A1_UP) + (unit * 4 + hw) * 512 + lane * 16;
    if (lane < 32) st16_dev(p.ws, ao, v4u{pa, T, pb_, T});
    STAMP(3);
  } else {
    lds_wait_ge<1>(lbase + C_S1, 4);
  }

  if (hw == 2 && bid == 0) {
    const v4u* gp = (const v4u*)p.gw + (lane & 7);
    v4u gv[9];
#pragma unroll
    for (int i = 0; i < 9; ++i) gv[i] = gp[8 * i];
    float sum = 0.f;
#pragma unroll
    for (int i = 0; i < 9; ++i) {
      const v4u a = *(LAS v4u*)(lds + L_X + (lane & 7) * 128 + (lane >> 3) * 16 + i * 1024);
#pragma unroll
      for (int e = 0; e < 4; ++e)
        sum = dot2(a[e], gv[i][e], sum);
    }
    sum += dpp<XOR1>(sum); sum += dpp<XOR2>(sum); sum += dpp<HMIR>(sum);
    const float gt = rcp(1.f + __expf(-sum));
    if ((lane & 7) == 0) st8_dev(p.ws, WS_GATE + (lane >> 3) * 8, v2u{__float_as_uint(gt), T});
  }
  if (hw == 3) {
    {
      const u32 so_ = WS_ST + (lane & 3) * 16;
      v4u sv;
      wait_until<4>(t_start + ST_AT);
      asm volatile("global_load_dwordx4 %0, %1, %2 sc1\n\ts_waitcnt vmcnt(0)" : "=&v"(sv) : "v"(so_), "s"(p.ws) : "memory");
      float dsum = (float)(int)(sv.x - t_start) + (float)(int)(sv.z - t_start);
      dsum += dpp<XOR1>(dsum); dsum += dpp<XOR2>(dsum);
      if (ballot_nz(tagbad(0u, sv, T)) == 0) *(LAS u32*)(lds + C_ORG) = t_start + (u32)(int)fminf(fmaxf(dsum * 0.125f, -200.f), 200.f);
    }
    {
      const u32 qo = WS_A1 + (16 * hk + (lane >> 2)) * 512 + bat * 64 + (lane & 3) * 16;
      v4u qv;
      wait_until<4>(lds_word(lbase + C_ORG) + Q_AT);
      NOUNROLL for (u32 spins = 0; spins < SPIN_CAP; ++spins) {
        asm volatile("global_load_dwordx4 %0, %1, %2 sc1\n\ts_waitcnt vmcnt(0)" : "=&v"(qv) : "v"(qo), "s"(p.ws) : "memory");
        if (ballot_nz(tagbad(0u, qv, T)) == 0) break;
        __builtin_amdgcn_s_sleep(8);
      }
      *(LAS v2u*)(lds + L_Q + (lane >> 4) * 128 + ((lane >> 1) & 1) * 64 + ((lane >> 2) & 3) * 16 + (lane & 1) * 8) = v2u{qv.x, qv.z};
      *(LAS u32*)(lds + C_T) = T;
      lds_inc(lbase + C_Q);
    }
    STAMP(4);
  }

  const u32 pst = lane * 16;
  if (isS) {
    if (hw < 2) {
      wait_until<8>(t_start + H_AT);
      const int u0 = (9 * rg) >> 1;
      const u32 ho = WS_A1 + A1_SG + u0 * 2048 + hw * 1024 + lane * 16 + 4096, uo = ho + (A1_UP - A1_SG), ho2 = ho + 4096, uo2 = uo + 4096;
      v4u h[10];
      NOUNROLL for (u32 spins = 0; spins < SPIN_CAP; ++spins) {
#define LH(i, a, o) "global_load_dwordx4 %" #i ", %" #a ", %14 offset:" #o " sc1\n\ts_sleep " H_GAP "\n\t"
        asm volatile(LH(0, 10, -4096) LH(1, 10, -2048) LH(2, 10, 0) LH(3, 10, 2048) LH(4, 12, 0)
                     LH(5, 11, -4096) LH(6, 11, -2048) LH(7, 11, 0) LH(8, 11, 2048) LH(9, 13, 0) "s_waitcnt vmcnt(0)"
                     : "=&v"(h[0]), "=&v"(h[1]), "=&v"(h[2]), "=&v"(h[3]), "=&v"(h[4]), "=&v"(h[5]), "=&v"(h[6]), "=&v"(h[7]), "=&v"(h[8]), "=&v"(h[9])
                     : "v"(ho), "v"(uo), "v"(ho2), "v"(uo2), "s"(p.ws) : "memory");
        u32 bad = 0;
#pragma unroll
        for (int i = 0; i < 10; ++i) bad = tagbad(bad, h[i], T);
        if (ballot_nz(bad) == 0) break;
        __builtin_amdgcn_s_sleep(8);
      }
#define HMUL(a, b) pk_bf16(lo_f(a) * lo_f(b), hi_f(a) * hi_f(b))
      const u32 hl = L_HA + ((lane >> 1) & 1) * 512 + (2 * (2 * hw + (lane >> 5)) + (lane & 1)) * 64 + ((lane >> 2) & 7) * 8;
#pragma unroll
      for (int k = 0; k < 5; ++k) *(LAS v2u*)(lds + hl + k * 1024) = v2u{HMUL(h[k].x, h[5 + k].x), HMUL(h[k].z, h[5 + k].z)};
      lds_inc(lbase + C_H);
      STAMP(6);
    }
    if (hw < 3) {
      lds_wait_ge<4>(lbase + C_H, 2);
      v4u A[9];
      u32 ha = L_HA + (rg & 1) * 512 + g * 64 + beta * 8;
      asm volatile("" : "+v"(ha));
#pragma unroll
      for (int ks = 0; ks < 9; ++ks) {
        const v2u a0 = *(LAS v2u*)(lds + ha + ks * 512), a1 = *(LAS v2u*)(lds + ha + ks * 512 + 256);
        A[ks] = v4u{a0.x, a0.y, a1.x, a1.y};
      }
      u32 ta[4];
      v4f acc[4];
#pragma unroll
      for (int j = 0; j < 4; ++j) { ta[j] = L_T1 + hw * 36864 + tr_lane_addr(lane, j); acc[j] = v4f{0.f, 0.f, 0.f, 0.f}; }
#define DSTEP(ks) _Pragma("unroll") for (int j = 0; j < 4; ++j) acc[j] = mfma(A[ks], trfrag(lds, ta[j], (ks) * 4096), acc[j])
      DSTEP(0); DSTEP(1); DSTEP(2); DSTEP(3); DSTEP(4); DSTEP(5);
      if (SKS == 7) { DSTEP(6); }
      u32 po = WS_P + (32 + rg) * P_ROW + (cg0 + hw) * 2048 + pst;
      asm volatile("" : "+v"(acc[0]), "+v"(acc[1]), "+v"(acc[2]), "+v"(acc[3]), "+v"(po));
      {
        const char* src = p.wd + (u32)(288 * rg + 32 * SKS) * 1152 + (cg0 + hw) * 128;
        const u32 so = drow * 1152 + dch * 16;
        u32 dst = lbase + L_T1 + hw * 36864 + 4 * SKS * 1024;
        wait_until<8>(t_start + T_LAST - 100);
        u32 tq = lds_word(lbase + C_ORG) + T_LAST + S2_DELAY;
        lds_wait_ge<1>(lbase + C_VI, 4);
        STAMP(5);
        NOUNROLL for (int i = 0; i < 4; ++i) {
          wait_until<0>(tq);
          tq += S2_GAP;
#pragma unroll
          for (int k = 0; k < 9 - SKS; ++k) { DMA16(so, src, dst, " nt"); src += 8 * 1152; dst += 1024; }
        }
      }
      if (SKS == 6) { VMCNT(8); DSTEP(6); }
      VMCNT(4); DSTEP(7);
      asm volatile("" : "+v"(acc[0]), "+v"(acc[1]), "+v"(acc[2]), "+v"(acc[3]));
      VMCNT(0);
      STAMP(9);
      DSTEP(8);
      st16_dev(p.ws, po, v4u{pk_bf16(acc[0][0], acc[0][1]), T, pk_bf16(acc[1][0], acc[1][1]), T});
      st16_dev(p.ws, po + 1024, v4u{pk_bf16(acc[2][0], acc[2][1]), T, pk_bf16(acc[3][0], acc[3][1]), T});
      STAMP(8);
    }
  } else if (hw < (jc < 10 ? 2 : 1)) {
    const int task = hw == 0 ? jc : 26 + jc, head = 4 * xc + task / 9, cg = task % 9, tau = xc * 36 + task;
    const u32 tb = L_WO + hw * 16384;
    v4u gg;
    {
      const u32 go = WS_GATE + ((tau >> 2) & 3) * 16;
      wait_until<8>(t_start + T_LAST - 350);
      NOUNROLL for (u32 spins = 0; spins < SPIN_CAP; ++spins) {
        asm volatile("global_load_dwordx4 %0, %1, %2 sc1\n\ts_waitcnt vmcnt(0)" : "=&v"(gg) : "v"(go), "s"(p.ws) : "memory");
        if (ballot_nz(tagbad(0u, gg, T)) == 0) break;
        __builtin_amdgcn_s_sleep(8);
      }
    }
    const u32 t_last = lds_word(lbase + C_ORG) + T_LAST;
    {
      const char* src = p.wo + (u32)(128 * head) * 1152 + cg * 128;
      const u32 so = (8 * (drow & 3) + (drow >> 2)) * 1152 + dch * 16;
      u32 dst = lbase + tb;
      wait_until<4>(t_last);
      lds_wait_ge<1>(lbase + C_VI, 4);
      STAMP(4);
      u32 tq = t_last + S2_DELAY;
      NOUNROLL for (int i = 0; i < 4; ++i) {
        wait_until<0>(tq);
        tq += S2_GAP;
#pragma unroll
        for (int k = 0; k < 4; ++k) { DMA16(so, src + 2 * k * 1152, dst, " nt"); dst += 1024; }
        src += 32 * 1152;
      }
    }
    u32 oo = WS_O + head * O_HEAD + lane * 16, oo2 = oo + 4096, lo = oo + 8192;
    u32 bp = 4 * (4 * n + g), bl = 4 * (16 * g + 4 * (n & 3));
    u32 po = WS_P + head * P_ROW + cg * 2048 + pst, po2 = po + 1024;
    u32 ro0 = WS_P + (lane >> 2) * P_ROW + tau * 64 + (lane & 3) * 16, ro1 = ro0 + 16 * P_ROW, ro2 = ro0 + 32 * P_ROW;
    const int nn = 4 * (tau & 3) + (lane & 3);
    u32 outo = ((2 * ((tau >> 2) & 3) + ((lane >> 2) & 1)) * 576 + (tau >> 5) * 64 + (nn >> 3) * 32 + ((tau >> 4) & 1) * 16 + (nn & 7) + ((lane >> 3) & 1) * 8) * 2;
    asm volatile("" : "+v"(bp), "+v"(bl));
    float g0 = __uint_as_float(gg.x), g1 = __uint_as_float(gg.z), h0 = 1.f - g0, h1 = 1.f - g1;
    u32 ta[4];
#pragma unroll
    for (int j = 0; j < 4; ++j) ta[j] = tb + tr_lane_addr(lane, j);
    asm volatile("" : "+v"(oo), "+v"(oo2), "+v"(lo), "+v"(po), "+v"(po2), "+v"(ro0), "+v"(ro1), "+v"(ro2), "+v"(outo), "+v"(ta[0]), "+v"(ta[1]), "+v"(ta[2]), "+v"(ta[3]),
                 "+v"(g0), "+v"(g1), "+v"(h0), "+v"(h1));
    lds_wait_ge<0>(lbase + C_O, 4);
    u32 x1 = now() + M1_OWN;
    if ((int)(x1 - (t_last + M1)) < 0) x1 = t_last + M1;
    u32 tries = 0;
    {
      v4u u[8], B[16];
      v2u e;
#define LO(i, a, o) "global_load_dwordx4 %" #i ", %" #a ", %12 offset:" #o "*1024 sc1\n\t"
#define SWEEP_O(C, PRE, POST) asm volatile(PRE "global_load_dwordx2 %8, %11, %12 sc1\n\t" LO(0, 9, 0) LO(1, 9, 1) LO(2, 9, 2) LO(3, 9, 3) LO(4, 10, 0) LO(5, 10, 1) LO(6, 10, 2) LO(7, 10, 3) POST \
                     : C(u[0]), C(u[1]), C(u[2]), C(u[3]), C(u[4]), C(u[5]), C(u[6]), C(u[7]), C(e) \
                     : "v"(oo), "v"(oo2), "v"(lo), "s"(p.ws), "s"(need) : "memory")
      u64 need = ~0ull;
      wait_until<0>(x1);
      STAMP(5);
      SWEEP_O("=&v", "", "s_waitcnt vmcnt(21)");
#define BREAD(ks) _Pragma("unroll") for (int j = 0; j < 4; ++j) B[4 * j + (ks)] = trfrag(lds, ta[j], (ks) * 4096)
      BREAD(0);
      VMCNT(17); BREAD(1);
      VMCNT(13); BREAD(2);
      VMCNT(9);
      STAMP(9);
      BREAD(3);
      __builtin_amdgcn_sched_barrier(0);
      v4f acc[4];
#define PULL(a, v) (u32)__builtin_amdgcn_ds_bpermute((int)(a), (int)(v))
#define OPULL(ks) A[ks] = v4u{PULL(bp, u[2 * (ks)].x), PULL(bp, u[2 * (ks)].z), PULL(bp, u[2 * (ks) + 1].x), PULL(bp, u[2 * (ks) + 1].z)}
      v4u A[4];
#define OSTEP(ks) do { const v4u A_ = A[ks]; \
        _Pragma("unroll") for (int j = 0; j < 4; ++j) acc[j] = mfma(A_, B[4 * j + (ks)], (ks) == 0 ? v4f{0.f, 0.f, 0.f, 0.f} : acc[j]); } while (0)
#define MIX(j) pk_bf16(fmaf(w0, acc[j][0], w1 * acc[j][1]), fmaf(w2, acc[j][2], w3 * acc[j][3]))
#define WEIGHTS() do { const float me = __uint_as_float(el), wme = rcp(1.f + __builtin_amdgcn_exp2f(dpp<XOR1>(me) - me)); \
        w0 = dpp<0x00>(wme); w1 = dpp<0x55>(wme); w2 = dpp<0xAA>(wme); w3 = dpp<0xFF>(wme); } while (0)
      float w0, w1, w2, w3;
      asm volatile("s_waitcnt vmcnt(6)" : "+v"(e), "+v"(u[0]), "+v"(u[1]) :: "memory");
      u32 el = PULL(bl, e.x);
      OPULL(0);
      __builtin_amdgcn_sched_barrier(0);
      asm volatile("s_waitcnt vmcnt(4)" : "+v"(u[2]), "+v"(u[3]) :: "memory");
      OPULL(1);
      __builtin_amdgcn_sched_barrier(0);
      asm volatile("s_waitcnt vmcnt(2)" : "+v"(u[4]), "+v"(u[5]) :: "memory");
      OPULL(2);
      __builtin_amdgcn_sched_barrier(0);
      asm volatile("s_waitcnt vmcnt(0)" : "+v"(u[6]), "+v"(u[7]) :: "memory");
      OPULL(3);
      __builtin_amdgcn_sched_barrier(0);
      STAMP(6);
      OSTEP(0); OSTEP(1); OSTEP(2); OSTEP(3);
      WEIGHTS();
      u32 bad = e.y ^ T;
#pragma unroll
      for (int i = 0; i < 8; ++i) bad = tagbad(bad, u[i], T);
      need = ballot_nz(bad);
      if (__builtin_expect(need != 0, 0)) {
        NOUNROLL for (u32 spins = 0; spins < SPIN_CAP; ++spins) {
          tries += 10;
          SWEEP_O("+v", "s_sleep 2\n\ts_mov_b64 exec, %13\n\t", "s_mov_b64 exec, -1\n\ts_waitcnt vmcnt(0)");
          bad = e.y ^ T;
#pragma unroll
          for (int i = 0; i < 8; ++i) bad = tagbad(bad, u[i], T);
          need = ballot_nz(bad);
          if (need == 0) break;
        }
        el = PULL(bl, e.x);
        OPULL(0); OPULL(1); OPULL(2); OPULL(3);
        OSTEP(0); OSTEP(1); OSTEP(2); OSTEP(3);
        WEIGHTS();
      }
      st16_dev(p.ws, po, v4u{MIX(0), T, MIX(1), T});
      st16_dev(p.ws, po2, v4u{MIX(2), T, MIX(3), T});
    }
    STAMP(7);
    {
      v4u a[3];
      u64 nd[3];
#define LP(i, o, m) "s_mov_b64 exec, %" #m "\n\tglobal_load_dwordx4 %" #i ", %" #o ", %6 sc1\n\t"
      u32 x2 = now() + M2_OWN;
      if ((int)(x2 - (t_last + M2)) < 0) x2 = t_last + M2;
      wait_until<0>(x2);
      STAMP(10);
      asm volatile("global_load_dwordx4 %0, %3, %6 sc1\n\tglobal_load_dwordx4 %1, %4, %6 sc1\n\tglobal_load_dwordx4 %2, %5, %6 sc1\n\ts_waitcnt vmcnt(0)"
                   : "=&v"(a[0]), "=&v"(a[1]), "=&v"(a[2]) : "v"(ro0), "v"(ro1), "v"(ro2), "s"(p.ws) : "memory");
      if (__builtin_expect(ballot_nz(tagbad(tagbad(tagbad(0u, a[0], T), a[1], T), a[2], T)) != 0, 0)) {
        NOUNROLL for (u32 spins = 0; spins < SPIN_CAP; ++spins) {
#pragma unroll
          for (int i = 0; i < 3; ++i) nd[i] = ballot_nz(tagbad(0u, a[i], T));
          if ((nd[0] | nd[1] | nd[2]) == 0) break;
          tries += 1;
          asm volatile("s_sleep 2\n\t" LP(0, 3, 7) LP(1, 4, 8) LP(2, 5, 9) "s_mov_b64 exec, -1\n\ts_waitcnt vmcnt(0)\n.Lfd_end:"
                       : "+v"(a[0]), "+v"(a[1]), "+v"(a[2]) : "v"(ro0), "v"(ro1), "v"(ro2), "s"(p.ws), "s"(nd[0]), "s"(nd[1]), "s"(nd[2]) : "memory");
        }
      }
      const float v00 = g0 * (lo_f(a[0].x) + lo_f(a[1].x)) + h0 * lo_f(a[2].x), v01 = g1 * (hi_f(a[0].x) + hi_f(a[1].x)) + h1 * hi_f(a[2].x);
      const float v10 = g0 * (lo_f(a[0].z) + lo_f(a[1].z)) + h0 * lo_f(a[2].z), v11 = g1 * (hi_f(a[0].z) + hi_f(a[1].z)) + h1 * hi_f(a[2].z);
      const bool b2 = (lane & 4) != 0, b3 = (lane & 8) != 0;
      const float c0 = (b2 ? v01 : v00) + dpp<ROR4>(b2 ? v00 : v01), c1 = (b2 ? v11 : v10) + dpp<ROR4>(b2 ? v10 : v11);
      const float r = xg_sum((b3 ? c1 : c0) + dpp<ROR8>(b3 ? c0 : c1));
      if (lane < 16) *(unsigned short*)(p.out + outo) = (unsigned short)pk_bf16(r, r);
      if (tau == 0 && lane == 0) __hip_atomic_store((u32*)(p.ws + WS_EPOCH), T, __ATOMIC_RELAXED, __HIP_MEMORY_SCOPE_AGENT);
    }
    STAMP(8);
    (void)tries;
  }
  STAMP_FLUSH();
}

extern "C" void kernel_launch(void* const* d_in, const int* in_sizes, int n_in, void* d_out, int out_size, void* d_ws, size_t ws_size,
                              hipStream_t stream) {
  Params p;
  p.x = (const char*)d_in[0]; p.k = (const char*)d_in[1]; p.v = (const char*)d_in[2]; p.wq = (const char*)d_in[3]; p.wo = (const char*)d_in[4];
  p.wg = (const char*)d_in[6]; p.wu = (const char*)d_in[7]; p.wd = (const char*)d_in[8]; p.gw = (const char*)d_in[9];
  p.out = (char*)d_out; p.ws = (char*)d_ws;
  hipLaunchKernelGGL(fused_decode, dim3(256), dim3(512), 0, stream, p);
}
```

```cpp
#define M2 252
#define M1 160
#include <hip/hip_runtime.h>
#include <stdint.h>

typedef unsigned u32;
typedef unsigned long long u64;
typedef u32 v4u __attribute__((ext_vector_type(4)));
typedef u32 v2u __attribute__((ext_vector_type(2)));
typedef float v4f __attribute__((ext_vector_type(4)));
typedef float v2f __attribute__((ext_vector_type(2)));
typedef short v4s __attribute__((ext_vector_type(4)));
typedef __bf16 v8bf __attribute__((ext_vector_type(8)));
typedef __bf16 v2bf __attribute__((ext_vector_type(2)));
#define LAS __attribute__((address_space(3)))
#define DEVINL __device__ __forceinline__
#define NOUNROLL _Pragma("nounroll")

#define S1_ORG 80
#define S1_DT 57
#define PACE_ORG 340
#define PACE_DT 58
#define ST_AT 470
#define Q_AT 600
#define H_AT 700
#define H_GAP "6"
#define S2_DELAY 50
#define S2_GAP 18
#define M1_OWN 20
#define M2_OWN 10
#define SKS 6
constexpr u32 T_LAST = PACE_ORG + 14 * PACE_DT + (3 * PACE_DT) / 4;

constexpr u32 WS_EPOCH = 0;
constexpr u32 WS_GATE = 256;
constexpr u32 WS_ST = 512;
constexpr u32 WS_A1 = 4096;
constexpr u32 A1_SG = 64 * 2048, A1_UP = (64 + 72) * 2048;
constexpr u32 WS_O = WS_A1 + 208 * 2048;
constexpr u32 O_HEAD = 9 * 1024;
constexpr u32 WS_P = WS_O + 32 * O_HEAD;
constexpr u32 P_ROW = 288 * 64;
constexpr u32 WS_DIAG = 4u << 20;
constexpr u32 L_T1 = 0;
constexpr u32 L_X = 73728;
constexpr u32 L_WO = L_X + 9216;
constexpr u32 L_HA = 110592;
constexpr u32 L_Q = 115712;
constexpr u32 L_ML = L_Q + 512;
constexpr u32 L_CTL = L_ML + 64;
constexpr u32 L_G = L_CTL + 64;
constexpr u32 L_OS = L_G + 128;
constexpr u32 L_SCR = L_OS + 4096;
constexpr u32 L_END = L_SCR + 1024;
constexpr u32 C_S1 = L_CTL + 0, C_Q = L_CTL + 4, C_ML = L_CTL + 8, C_O = L_CTL + 12, C_VI = L_CTL + 16, C_H = L_CTL + 20, C_X = L_CTL + 24,
              C_D3 = L_CTL + 28, C_T = L_CTL + 32, C_ORG = L_CTL + 40;
constexpr u32 SPIN_CAP = 1u << 17;

struct Params {
  const char* k; const char* v; const char* x; const char* wq; const char* wo; const char* wg; const char* wu; const char* wd;
  const char* gw; char* out; char* ws;
};

DEVINL v4f mfma(v4u a, v4u b, v4f c) {
  return __builtin_amdgcn_mfma_f32_16x16x32_bf16(__builtin_bit_cast(v8bf, a), __builtin_bit_cast(v8bf, b), c, 0, 0, 0);
}
DEVINL u32 pk_bf16(float lo, float hi) { v2f f = {lo, hi}; return __builtin_bit_cast(u32, __builtin_convertvector(f, v2bf)); }
DEVINL float bf16r(float f) { return __builtin_bit_cast(float, pk_bf16(f, f) & 0xffff0000u); }
DEVINL float lo_f(u32 v) { return __uint_as_float(v << 16); }
DEVINL float hi_f(u32 v) { return __uint_as_float(v & 0xffff0000u); }
DEVINL float dot2(u32 a, u32 b, float c) { return __builtin_amdgcn_fdot2_f32_bf16(__builtin_bit_cast(v2bf, a), __builtin_bit_cast(v2bf, b), c, false); }
DEVINL float rcp(float x) { return __builtin_amdgcn_rcpf(x); }
template <int CTRL> DEVINL float dpp(float x) {
  return __builtin_bit_cast(float, __builtin_amdgcn_mov_dpp(__builtin_bit_cast(int, x), CTRL, 0xf, 0xf, true));
}
constexpr int XOR1 = 0xB1, XOR2 = 0x4E, ROR4 = 0x124, ROR8 = 0x128, HMIR = 0x141;
DEVINL float xg_max(float x) {
  auto s = __builtin_amdgcn_permlane16_swap(__float_as_uint(x), __float_as_uint(x), false, false);
  x = fmaxf(__uint_as_float(s[0]), __uint_as_float(s[1]));
  auto t = __builtin_amdgcn_permlane32_swap(__float_as_uint(x), __float_as_uint(x), false, false);
  return fmaxf(__uint_as_float(t[0]), __uint_as_float(t[1]));
}
DEVINL float xg_sum(float x) {
  auto s = __builtin_amdgcn_permlane16_swap(__float_as_uint(x), __float_as_uint(x), false, false);
  x = __uint_as_float(s[0]) + __uint_as_float(s[1]);
  auto t = __builtin_amdgcn_permlane32_swap(__float_as_uint(x), __float_as_uint(x), false, false);
  return __uint_as_float(t[0]) + __uint_as_float(t[1]);
}
DEVINL u32 tagbad(u32 acc, v4u u, u32 T) {
  acc = __builtin_amdgcn_bitop3_b32(acc, u.y, T, 0xF6);
  return __builtin_amdgcn_bitop3_b32(acc, u.w, T, 0xF6);
}
DEVINL u64 ballot_nz(u32 v) { return __builtin_amdgcn_ballot_w64(v != 0); }
DEVINL void lds_inc(u32 addr) {
  const u32 one = 1;
  asm volatile("s_mov_b64 exec, 1\n\tds_add_u32 %0, %1\n\ts_mov_b64 exec, -1" :: "v"(addr), "v"(one) : "memory");
}
DEVINL u32 lds_word(u32 addr) {
  u32 r;
  asm volatile("ds_read_b32 %0, %1\n\ts_waitcnt lgkmcnt(0)" : "=v"(r) : "v"(addr) : "memory");
  return __builtin_amdgcn_readfirstlane(r);
}
template <int SLEEP> DEVINL void lds_wait_ge(u32 addr, u32 want) {
  NOUNROLL for (u32 spins = 0; spins < (SPIN_CAP << 5); ++spins) {
    if (lds_word(addr) >= want) break;
    if (SLEEP) __builtin_amdgcn_s_sleep(SLEEP);
  }
}
DEVINL u32 now() { return (u32)__builtin_amdgcn_s_memrealtime(); }
template <int SLEEP> DEVINL void wait_until(u32 t) {
  NOUNROLL while ((int)(now() - t) < 0) { if (SLEEP) __builtin_amdgcn_s_sleep(SLEEP); }
}
#define DMA16(voff, sbase, ldsdst, POL) \
  asm volatile("s_mov_b32 m0, %2\n\ts_nop 0\n\tglobal_load_lds_dwordx4 %0, %1" POL :: "v"(voff), "s"(sbase), "s"(ldsdst) : "memory")
#define VMCNT_(n) asm volatile("s_waitcnt vmcnt(" #n ")" ::: "memory")
#define VMCNT(n) VMCNT_(n)
DEVINL void st16_dev(char* base, u32 off, v4u d) { asm volatile("global_store_dwordx4 %0, %1, %2 sc1\n\ts_nop 1" :: "v"(off), "v"(d), "s"(base) : "memory"); }
DEVINL void st8_dev(char* base, u32 off, v2u d) { asm volatile("global_store_dwordx2 %0, %1, %2 sc1" :: "v"(off), "v"(d), "s"(base) : "memory"); }
DEVINL v4u trfrag(LAS char* l, u32 a0, u32 imm) {
  const v2u x = __builtin_bit_cast(v2u, __builtin_amdgcn_ds_read_tr16_b64_v4i16((LAS v4s*)(l + a0 + imm)));
  const v2u y = __builtin_bit_cast(v2u, __builtin_amdgcn_ds_read_tr16_b64_v4i16((LAS v4s*)(l + a0 + imm + 2048)));
  return v4u{x.x, x.y, y.x, y.y};
}
DEVINL u32 tr_lane_addr(int lane, int j) {
  const int r0 = (lane >> 2) & 15, p = lane & 3;
  const int c = (p < 2 ? j : j + 4) ^ ((r0 >> 1) & 3);
  return r0 * 128 + c * 16 + 8 * (p & 1);
}

#define STAMP(i)
#define STAMP_DECL
#define STAMP_FLUSH()

__global__ void __launch_bounds__(512, 2) fused_decode(Params p) {
  __shared__ __attribute__((aligned(1024))) char lds_raw[L_END];
  LAS char* lds = (LAS char*)lds_raw;
  const u32 lbase = (u32)(uintptr_t)lds;
  const int tid = threadIdx.x, lane = tid & 63, wave = __builtin_amdgcn_readfirstlane(tid >> 6);
  const int bid = blockIdx.x, xc = bid & 7, jc = bid >> 3;
  const int hk = 2 * xc + (jc & 1), half = (jc >> 1) & 1, bat = jc >> 2;
  const int g = lane >> 4, n = lane & 15;
  STAMP_DECL;
  const u64 t_start64 = __builtin_amdgcn_s_memrealtime();
  u32 rot = bid; rot *= 0x9E3779B1u; rot ^= rot >> 15; rot *= 0x85EBCA77u; rot >>= 13;
  u32 reg = ((u32)(bat * 16 + hk) * 4096 + 3072 + half * 512 + ((wave + (rot >> 3)) & 3) * 16) * 256;
  rot &= 7;
  constexpr u32 TSTR = 16384;
  u32 reg0 = reg + rot * TSTR;
  u32 koff = (n >> 2) * 256 + g * 64 + (n & 3) * 16;
  asm volatile("" : "+s"(reg), "+s"(rot), "+s"(reg0));
  asm volatile("" : "+v"(koff));
  {
    const char* pc; u32 len;
    asm volatile("s_getpc_b64 %0\n.Lfd_anchor:\n\ts_mov_b32 %1, .Lfd_end-.Lfd_anchor" : "=s"(pc), "=s"(len));
    const u32 po_ = (u32)(jc * 8 + wave) * 1024u;
    if (po_ < len) { const u32 lo_ = lane * 16; DMA16(lo_, (const char*)((uintptr_t)(pc + min(po_, len - 1024u)) & ~(uintptr_t)15), lbase + L_SCR, ""); }
  }
  asm volatile("" :: "s"(p.x), "s"(p.k), "s"(p.v), "s"(p.wq), "s"(p.wo), "s"(p.wg), "s"(p.wu), "s"(p.wd), "s"(p.gw), "s"(p.out), "s"(p.ws), "s"(t_start64));
  const u32 t_start = (u32)t_start64;

  if (__builtin_expect(wave < 4, 1)) {
    const char* kb = p.k + reg;
    const char* vb = p.v + reg;
    const u32 voff = lane * 16;
    v4u K[32], V[32];
#define LD4(A, b_, off, t) do { \
    asm volatile("global_load_dwordx4 %0, %4, %5 nt\n\tglobal_load_dwordx4 %1, %4, %5 offset:1024 nt\n\t" \
                 "global_load_dwordx4 %2, %4, %5 offset:2048 nt\n\tglobal_load_dwordx4 %3, %4, %5 offset:3072 nt" \
                 : "=&v"(A[4 * (t)]), "=&v"(A[4 * (t) + 1]), "=&v"(A[4 * (t) + 2]), "=&v"(A[4 * (t) + 3]) : "v"(off), "s"(b_) : "memory"); } while (0)
#define LDK(t) LD4(K, kb + (((t) + rot) & 7) * TSTR, koff, t)
#define LDV(t) LD4(V, vb + (((t) + rot) & 7) * TSTR, voff, t)
#define TIE4(A, t) asm volatile("" : "+v"(A[4 * (t)]), "+v"(A[4 * (t) + 1]), "+v"(A[4 * (t) + 2]), "+v"(A[4 * (t) + 3]))
    LD4(K, p.k + reg0, koff, 0);
    STAMP(1);
    if (tid < 16) *(LAS u32*)(lds + L_CTL + lane * 4) = lane == 10 ? t_start : 0u;
    asm volatile("s_waitcnt lgkmcnt(0)" ::: "memory");
    __builtin_amdgcn_s_barrier();
    const u32 t_off = PACE_ORG + (wave * PACE_DT) / 4, a_org = lbase + C_ORG;
#define PACE(m) wait_until<1>(lds_word(a_org) + t_off + (m) * PACE_DT)
    PACE(0); LDK(1); PACE(1); LDK(2); PACE(2); LDK(3); PACE(3); LDK(4); PACE(4); LDK(5); PACE(5); LDK(6); PACE(6); LDK(7);
    PACE(7); LDV(0); PACE(8); LDV(1); PACE(9); LDV(2);
    STAMP(2);
    lds_wait_ge<1>(lbase + C_Q, 1);
    STAMP(3);
    const v4u qf = *(LAS v4u*)(lds + L_Q + (n >> 3) * 256 + (4 * g + (n & 3)) * 16);
    const u32 T = *(LAS u32*)(lds + C_T);
    float s[32];
    const v4f zero = {0.f, 0.f, 0.f, 0.f};
#define QK(i) do { const v4f d_ = mfma(K[i], qf, zero); s[i] = ((dpp<0x00>(d_[0]) + dpp<0x55>(d_[1])) + dpp<0xAA>(d_[2])) + dpp<0xFF>(d_[3]); } while (0)
    VMCNT(12);
    TIE4(K, 0); TIE4(K, 1); TIE4(K, 2); TIE4(K, 3);
#pragma unroll
    for (int i = 0; i < 16; ++i) QK(i);
    TIE4(s, 0); TIE4(s, 1); TIE4(s, 2); TIE4(s, 3);
    PACE(10); LDV(3);
    TIE4(K, 4); TIE4(K, 5); TIE4(K, 6); TIE4(K, 7);
#pragma unroll
    for (int i = 16; i < 32; ++i) QK(i);
    constexpr float C2 = 0.08838834764831845f * 1.4426950408889634f;
    float mx = s[0];
#pragma unroll
    for (int i = 1; i < 32; ++i) mx = fmaxf(mx, s[i]);
    mx = xg_max(mx) * C2;
    asm volatile("" : "+v"(mx));
    PACE(11); LDV(4);
    asm volatile("" : "+v"(mx));
    float ls = 0.f;
#pragma unroll
    for (int i = 0; i < 32; ++i) { s[i] = __builtin_amdgcn_exp2f(fmaf(s[i], C2, -mx)); ls += s[i]; }
    ls = xg_sum(ls);
    if ((lane & 0x37) == 0) *(LAS v2f*)(lds + L_ML + wave * 16 + (lane >> 3) * 8) = v2f{mx, ls};
    lds_inc(lbase + C_ML);
    PACE(12); LDV(5);
    u32 msk[4];
#pragma unroll
    for (int i = 0; i < 4; ++i) msk[i] = (((lane & 7) >> 1) == i) ? ((lane & 1) ? 0xffff0000u : 0x0000ffffu) : 0u;
    v4f acc = zero;
    VMCNT(8);
    TIE4(V, 0); TIE4(V, 1); TIE4(V, 2); TIE4(V, 3);
#pragma unroll
    for (int i = 0; i < 16; ++i) {
      const u32 pp = pk_bf16(s[i], s[i]);
      acc = mfma(V[i], v4u{pp & msk[0], pp & msk[1], pp & msk[2], pp & msk[3]}, acc);
    }
    asm volatile("" : "+v"(acc));
    PACE(13); LDV(6);
    TIE4(s, 4); TIE4(s, 5); TIE4(s, 6); TIE4(s, 7);
    v4u pb[16];
#pragma unroll
    for (int i = 0; i < 16; ++i) {
      const u32 pp = pk_bf16(s[16 + i], s[16 + i]);
      pb[i] = v4u{pp & msk[0], pp & msk[1], pp & msk[2], pp & msk[3]};
    }
    TIE4(pb, 0); TIE4(pb, 1); TIE4(pb, 2); TIE4(pb, 3);
    PACE(14); LDV(7);
    lds_inc(lbase + C_VI);
    __builtin_amdgcn_s_setprio(3);
    STAMP(4);
    lds_wait_ge<1>(lbase + C_ML, 4);
    float fw;
    u32 lse;
    {
      v2f ml[4];
#pragma unroll
      for (int w = 0; w < 4; ++w) ml[w] = *(LAS v2f*)(lds + L_ML + w * 16 + (n >> 3) * 8);
      const float M = fmaxf(fmaxf(ml[0].x, ml[1].x), fmaxf(ml[2].x, ml[3].x));
      float L = 0.f;
#pragma unroll
      for (int w = 0; w < 4; ++w) L = fmaf(ml[w].y, __builtin_amdgcn_exp2f(ml[w].x - M), L);
      fw = __builtin_amdgcn_exp2f(mx - M) * rcp(L);
      lse = __float_as_uint(M + __builtin_amdgcn_logf(L));
    }
    u32 a_own = lbase + L_OS + wave * 1024 + lane * 16, a_all = lbase + L_OS + lane * 16, a_cnt = lbase + C_O, one = 1;
    u32 ooff = WS_O + (2 * hk + (n >> 3)) * O_HEAD + (2 * g + ((n >> 2) & 1)) * 1024 + (2 * bat + half) * 64 + (n & 3) * 16;
    u32 loff = WS_O + (2 * hk + (n >> 3)) * O_HEAD + 8192 + (2 * bat + half) * 64 + (n & 3) * 16;
    v4u lsev = {lse, T, lse, T};
    asm volatile("" : "+v"(fw), "+v"(acc), "+v"(a_own), "+v"(a_all), "+v"(a_cnt), "+v"(one), "+v"(ooff), "+v"(loff), "+v"(lsev));
    STAMP(5);
    VMCNT(12); TIE4(V, 4);
#pragma unroll
    for (int i = 0; i < 4; ++i) acc = mfma(V[16 + i], pb[i], acc);
    asm volatile("" : "+v"(acc));
    VMCNT(8); TIE4(V, 5);
#pragma unroll
    for (int i = 4; i < 8; ++i) acc = mfma(V[16 + i], pb[i], acc);
    asm volatile("" : "+v"(acc));
    VMCNT(4); TIE4(V, 6);
#pragma unroll
    for (int i = 8; i < 12; ++i) acc = mfma(V[16 + i], pb[i], acc);
    asm volatile("" : "+v"(acc));
    VMCNT(2); asm volatile("" : "+v"(V[28]), "+v"(V[29]));
    acc = mfma(V[28], pb[12], acc); acc = mfma(V[29], pb[13], acc);
    asm volatile("" : "+v"(acc));
    VMCNT(0); asm volatile("" : "+v"(V[30]), "+v"(V[31]));
    acc = mfma(V[30], pb[14], acc); acc = mfma(V[31], pb[15], acc);
    STAMP(6);
    acc *= fw;
    {
      u32 tk; v4f o0, o1, o2, o3;
      asm volatile("ds_write_b128 %5, %6\n\t"
                   "s_mov_b64 exec, 1\n\tds_add_rtn_u32 %0, %7, %8\n\ts_mov_b64 exec, -1\n\t"
                   "ds_read_b128 %1, %9\n\tds_read_b128 %2, %9 offset:1024\n\tds_read_b128 %3, %9 offset:2048\n\tds_read_b128 %4, %9 offset:3072\n\t"
                   "s_waitcnt lgkmcnt(0)"
                   : "=&v"(tk), "=&v"(o0), "=&v"(o1), "=&v"(o2), "=&v"(o3)
                   : "v"(a_own), "v"(acc), "v"(a_cnt), "v"(one), "v"(a_all) : "memory");
      if (__builtin_amdgcn_readfirstlane(tk) == 3) {
        const v4f o = (o0 + o1) + (o2 + o3);
        v4u ov = {pk_bf16(o[0], o[1]), T, pk_bf16(o[2], o[3]), T};
        asm volatile("" : "+v"(ov));
        asm volatile("s_mov_b64 exec, 0x0f0f\n\tglobal_store_dwordx4 %0, %1, %2 sc1\n\ts_mov_b64 exec, -1" :: "v"(loff), "v"(lsev), "s"(p.ws) : "memory");
        st16_dev(p.ws, ooff, ov);
      }
    }
    STAMP(7);
    STAMP_FLUSH();
    return;
  }

  const int hw = wave - 4;
  __builtin_amdgcn_s_setprio(3);
  u32 ep;
  { const u32 z = 0; asm volatile("global_load_dword %0, %1, %2 sc1" : "=v"(ep) : "v"(z), "s"(p.ws) : "memory"); }
  __builtin_amdgcn_s_barrier();
  const bool isS = jc >= 26;
  const int kind = jc < 8 ? 0 : jc < 17 ? 1 : 2;
  const int unit = kind == 0 ? xc * 8 + jc : xc * 9 + jc - (kind == 1 ? 8 : 17);
  const int us = xc * 6 + (jc - 26), rg = us / 3, cg0 = 3 * (us % 3);
  const int drow = lane >> 3, dch = (lane & 7) ^ ((drow >> 1) & 3);
  const int beta = 2 * (n >> 2) + (n & 1);
  if (!isS) {
    const u32 xo = (lane & 7) * 1152 + (lane >> 3) * 16;
    DMA16(xo, p.x + hw * 128, lbase + L_X + hw * 1024, "");
    DMA16(xo, p.x + (hw + 4) * 128, lbase + L_X + (hw + 4) * 1024, "");
    if (hw == 0) DMA16(xo, p.x + 8 * 128, lbase + L_X + 8 * 1024, "");
  }
  STAMP(1);
  {
    const u32 rowb = isS ? 1152 : kind == 0 ? 8192 : 9216;
    const char* src = isS ? p.wd + (u32)(288 * rg + 8 * SKS * hw) * 1152 + cg0 * 128
                          : (kind == 0 ? p.wq : kind == 1 ? p.wg : p.wu) + (u32)(144 * hw) * rowb + unit * 128;
    u32 so = drow * rowb + dch * 16;
    u32 sa = isS ? 128 : 8 * rowb, sb = isS ? 8 * 1152 - 256 : 8 * rowb, da = isS ? 36864 : 1024, db = isS ? 1024 - 2 * 36864 : 1024;
    u32 dst = lbase + L_T1 + (isS ? SKS : 18) * hw * 1024;
    const u32 dt1 = isS ? (S1_DT * 6) / SKS : S1_DT;
    u32 tq = (t_start + S1_ORG) * 4;
#define S1WAIT() do { NOUNROLL while ((int)(now() * 4 - tq) < 0) __builtin_amdgcn_s_sleep(1); tq += dt1; } while (0)
    NOUNROLL for (int i = 0; i < (isS ? SKS : 6); ++i) {
      S1WAIT(); DMA16(so, src, dst, " nt"); src += sa; dst += da;
      S1WAIT(); DMA16(so, src, dst, " nt"); src += sa; dst += da;
      S1WAIT(); DMA16(so, src, dst, " nt"); src += sb; dst += db;
    }
  }
  VMCNT(18);
  u32 T;
  asm volatile("v_readfirstlane_b32 %0, %1\n\ts_nop 1" : "=s"(T) : "v"(ep) : "memory");
  T += 1u;
  lds_inc(lbase + C_X);
  if (bid < 8 && hw == 3 && lane == 0) st8_dev(p.ws, WS_ST + bid * 8, v2u{t_start, T});
  v4u xf[18];
  if (!isS) {
    lds_wait_ge<1>(lbase + C_X, 4);
    u32 xa = L_X + (g >> 1) * 128 + beta * 16 + (g & 1) * 8;
    asm volatile("" : "+v"(xa));
#pragma unroll
    for (int ks = 0; ks < 18; ++ks) {
      const v2u x0 = *(LAS v2u*)(lds + xa + ks * 512), x1 = *(LAS v2u*)(lds + xa + ks * 512 + 256);
      xf[ks] = v4u{x0.x, x0.y, x1.x, x1.y};
    }
  }
  VMCNT(0);
  lds_inc(lbase + C_S1);
  if (!isS) {
    u32 ta = L_T1 + tr_lane_addr(lane, hw), tb2 = ta + 9 * 4096;
    asm volatile("" : "+v"(ta), "+v"(tb2));
    lds_wait_ge<1>(lbase + C_S1, 4);
    STAMP(2);
    v4u bf[18];
#pragma unroll
    for (int ks = 0; ks < 18; ++ks) bf[ks] = ks < 9 ? trfrag(lds, ta, ks * 4096) : trfrag(lds, tb2, (ks - 9) * 4096);
    __builtin_amdgcn_sched_barrier(0);
    v4f acc = {0.f, 0.f, 0.f, 0.f}, acc2 = {0.f, 0.f, 0.f, 0.f};
#pragma unroll
    for (int ks = 0; ks < 18; ks += 2) { acc = mfma(xf[ks], bf[ks], acc); acc2 = mfma(xf[ks + 1], bf[ks + 1], acc2); }
    acc += acc2;
    float a0 = acc[0], a1 = acc[1];
    if (kind == 1) {
      a0 = bf16r(a0); a1 = bf16r(a1);
      a0 = a0 * rcp(1.f + __expf(-a0)); a1 = a1 * rcp(1.f + __expf(-a1));
    }
    const float n0 = dpp<XOR1>(a0), n1 = dpp<XOR1>(a1);
    const u32 pk = (n & 1) ? pk_bf16(n1, a1) : pk_bf16(a0, n0);
    const int sl_ = 16 * (lane >> 3) + 4 * (lane & 3) + ((lane >> 2) & 1);
    const u32 pa = (u32)__builtin_amdgcn_ds_bpermute(4 * (sl_ & 63), (int)pk), pb_ = (u32)__builtin_amdgcn_ds_bpermute(4 * ((sl_ + 2) & 63), (int)pk);
    const u32 ao = WS_A1 + (kind == 0 ? 0 : kind == 1 ? A1_SG : A1_UP) + (unit * 4 + hw) * 512 + lane * 16;
    if (lane < 32) st16_dev(p.ws, ao, v4u{pa, T, pb_, T});
    STAMP(3);
  } else {
    lds_wait_ge<1>(lbase + C_S1, 4);
  }

  if (hw == 2 && bid == 0) {
    const v4u* gp = (const v4u*)p.gw + (lane & 7);
    v4u gv[9];
#pragma unroll
    for (int i = 0; i < 9; ++i) gv[i] = gp[8 * i];
    float sum = 0.f;
#pragma unroll
    for (int i = 0; i < 9; ++i) {
      const v4u a = *(LAS v4u*)(lds + L_X + (lane & 7) * 128 + (lane >> 3) * 16 + i * 1024);
#pragma unroll
      for (int e = 0; e < 4; ++e)
        sum = dot2(a[e], gv[i][e], sum);
    }
    sum += dpp<XOR1>(sum); sum += dpp<XOR2>(sum); sum += dpp<HMIR>(sum);
    const float gt = rcp(1.f + __expf(-sum));
    if ((lane & 7) == 0) st8_dev(p.ws, WS_GATE + (lane >> 3) * 8, v2u{__float_as_uint(gt), T});
  }
  if (hw == 3) {
    {
      const u32 so_ = WS_ST + (lane & 3) * 16;
      v4u sv;
      wait_until<4>(t_start + ST_AT);
      asm volatile("global_load_dwordx4 %0, %1, %2 sc1\n\ts_waitcnt vmcnt(0)" : "=&v"(sv) : "v"(so_), "s"(p.ws) : "memory");
      float dsum = (float)(int)(sv.x - t_start) + (float)(int)(sv.z - t_start);
      dsum += dpp<XOR1>(dsum); dsum += dpp<XOR2>(dsum);
      if (ballot_nz(tagbad(0u, sv, T)) == 0) *(LAS u32*)(lds + C_ORG) = t_start + (u32)(int)fminf(fmaxf(dsum * 0.125f, -200.f), 200.f);
    }
    {
      const u32 qo = WS_A1 + (16 * hk + (lane >> 2)) * 512 + bat * 64 + (lane & 3) * 16;
      v4u qv;
      wait_until<4>(lds_word(lbase + C_ORG) + Q_AT);
      NOUNROLL for (u32 spins = 0; spins < SPIN_CAP; ++spins) {
        asm volatile("global_load_dwordx4 %0, %1, %2 sc1\n\ts_waitcnt vmcnt(0)" : "=&v"(qv) : "v"(qo), "s"(p.ws) : "memory");
        if (ballot_nz(tagbad(0u, qv, T)) == 0) break;
        __builtin_amdgcn_s_sleep(8);
      }
      *(LAS v2u*)(lds + L_Q + (lane >> 4) * 128 + ((lane >> 1) & 1) * 64 + ((lane >> 2) & 3) * 16 + (lane & 1) * 8) = v2u{qv.x, qv.z};
      *(LAS u32*)(lds + C_T) = T;
      lds_inc(lbase + C_Q);
    }
    STAMP(4);
  }

  const u32 pst = lane * 16;
  if (isS) {
    if (hw < 2) {
      wait_until<8>(t_start + H_AT);
      const int u0 = (9 * rg) >> 1;
      const u32 ho = WS_A1 + A1_SG + u0 * 2048 + hw * 1024 + lane * 16 + 4096, uo = ho + (A1_UP - A1_SG), ho2 = ho + 4096, uo2 = uo + 4096;
      v4u h[10];
      NOUNROLL for (u32 spins = 0; spins < SPIN_CAP; ++spins) {
#define LH(i, a, o) "global_load_dwordx4 %" #i ", %" #a ", %14 offset:" #o " sc1\n\ts_sleep " H_GAP "\n\t"
        asm volatile(LH(0, 10, -4096) LH(1, 10, -2048) LH(2, 10, 0) LH(3, 10, 2048) LH(4, 12, 0)
                     LH(5, 11, -4096) LH(6, 11, -2048) LH(7, 11, 0) LH(8, 11, 2048) LH(9, 13, 0) "s_waitcnt vmcnt(0)"
                     : "=&v"(h[0]), "=&v"(h[1]), "=&v"(h[2]), "=&v"(h[3]), "=&v"(h[4]), "=&v"(h[5]), "=&v"(h[6]), "=&v"(h[7]), "=&v"(h[8]), "=&v"(h[9])
                     : "v"(ho), "v"(uo), "v"(ho2), "v"(uo2), "s"(p.ws) : "memory");
        u32 bad = 0;
#pragma unroll
        for (int i = 0; i < 10; ++i) bad = tagbad(bad, h[i], T);
        if (ballot_nz(bad) == 0) break;
        __builtin_amdgcn_s_sleep(8);
      }
#define HMUL(a, b) pk_bf16(lo_f(a) * lo_f(b), hi_f(a) * hi_f(b))
      const u32 hl = L_HA + ((lane >> 1) & 1) * 512 + (2 * (2 * hw + (lane >> 5)) + (lane & 1)) * 64 + ((lane >> 2) & 7) * 8;
#pragma unroll
      for (int k = 0; k < 5; ++k) *(LAS v2u*)(lds + hl + k * 1024) = v2u{HMUL(h[k].x, h[5 + k].x), HMUL(h[k].z, h[5 + k].z)};
      lds_inc(lbase + C_H);
      STAMP(6);
    }
    if (hw < 3) {
      lds_wait_ge<4>(lbase + C_H, 2);
      v4u A[9];
      u32 ha = L_HA + (rg & 1) * 512 + g * 64 + beta * 8;
      asm volatile("" : "+v"(ha));
#pragma unroll
      for (int ks = 0; ks < 9; ++ks) {
        const v2u a0 = *(LAS v2u*)(lds + ha + ks * 512), a1 = *(LAS v2u*)(lds + ha + ks * 512 + 256);
        A[ks] = v4u{a0.x, a0.y, a1.x, a1.y};
      }
      u32 ta[4];
      v4f acc[4];
#pragma unroll
      for (int j = 0; j < 4; ++j) { ta[j] = L_T1 + hw * 36864 + tr_lane_addr(lane, j); acc[j] = v4f{0.f, 0.f, 0.f, 0.f}; }
#define DSTEP(ks) _Pragma("unroll") for (int j = 0; j < 4; ++j) acc[j] = mfma(A[ks], trfrag(lds, ta[j], (ks) * 4096), acc[j])
      DSTEP(0); DSTEP(1); DSTEP(2); DSTEP(3); DSTEP(4); DSTEP(5);
      if (SKS == 7) { DSTEP(6); }
      u32 po = WS_P + (32 + rg) * P_ROW + (cg0 + hw) * 2048 + pst;
      asm volatile("" : "+v"(acc[0]), "+v"(acc[1]), "+v"(acc[2]), "+v"(acc[3]), "+v"(po));
      {
        const char* src = p.wd + (u32)(288 * rg + 32 * SKS) * 1152 + (cg0 + hw) * 128;
        const u32 so = drow * 1152 + dch * 16;
        u32 dst = lbase + L_T1 + hw * 36864 + 4 * SKS * 1024;
        wait_until<8>(t_start + T_LAST - 100);
        u32 tq = lds_word(lbase + C_ORG) + T_LAST + S2_DELAY;
        lds_wait_ge<1>(lbase + C_VI, 4);
        STAMP(5);
        NOUNROLL for (int i = 0; i < 4; ++i) {
          wait_until<0>(tq);
          tq += S2_GAP;
#pragma unroll
          for (int k = 0; k < 9 - SKS; ++k) { DMA16(so, src, dst, " nt"); src += 8 * 1152; dst += 1024; }
        }
      }
      if (SKS == 6) { VMCNT(8); DSTEP(6); }
      VMCNT(4); DSTEP(7);
      asm volatile("" : "+v"(acc[0]), "+v"(acc[1]), "+v"(acc[2]), "+v"(acc[3]));
      VMCNT(0);
      STAMP(9);
      DSTEP(8);
      st16_dev(p.ws, po, v4u{pk_bf16(acc[0][0], acc[0][1]), T, pk_bf16(acc[1][0], acc[1][1]), T});
      st16_dev(p.ws, po + 1024, v4u{pk_bf16(acc[2][0], acc[2][1]), T, pk_bf16(acc[3][0], acc[3][1]), T});
      STAMP(8);
    }
  } else if (hw < (jc < 10 ? 2 : 1)) {
    const int task = hw == 0 ? jc : 26 + jc, head = 4 * xc + task / 9, cg = task % 9, tau = xc * 36 + task;
    const u32 tb = L_WO + hw * 16384;
    v4u gg;
    {
      const u32 go = WS_GATE + ((tau >> 2) & 3) * 16;
      wait_until<8>(t_start + T_LAST - 350);
      NOUNROLL for (u32 spins = 0; spins < SPIN_CAP; ++spins) {
        asm volatile("global_load_dwordx4 %0, %1, %2 sc1\n\ts_waitcnt vmcnt(0)" : "=&v"(gg) : "v"(go), "s"(p.ws) : "memory");
        if (ballot_nz(tagbad(0u, gg, T)) == 0) break;
        __builtin_amdgcn_s_sleep(8);
      }
    }
    const u32 t_last = lds_word(lbase + C_ORG) + T_LAST;
    {
      const char* src = p.wo + (u32)(128 * head) * 1152 + cg * 128;
      const u32 so = (8 * (drow & 3) + (drow >> 2)) * 1152 + dch * 16;
      u32 dst = lbase + tb;
      wait_until<4>(t_last);
      lds_wait_ge<1>(lbase + C_VI, 4);
      STAMP(4);
      u32 tq = t_last + S2_DELAY;
      NOUNROLL for (int i = 0; i < 4; ++i) {
        wait_until<0>(tq);
        tq += S2_GAP;
#pragma unroll
        for (int k = 0; k < 4; ++k) { DMA16(so, src + 2 * k * 1152, dst, " nt"); dst += 1024; }
        src += 32 * 1152;
      }
    }
    u32 oo = WS_O + head * O_HEAD + lane * 16, oo2 = oo + 4096, lo = oo + 8192;
    u32 bp = 4 * (4 * n + g), bl = 4 * (16 * g + 4 * (n & 3));
    u32 po = WS_P + head * P_ROW + cg * 2048 + pst, po2 = po + 1024;
    u32 ro0 = WS_P + (lane >> 2) * P_ROW + tau * 64 + (lane & 3) * 16, ro1 = ro0 + 16 * P_ROW, ro2 = ro0 + 32 * P_ROW;
    const int nn = 4 * (tau & 3) + (lane & 3);
    u32 outo = ((2 * ((tau >> 2) & 3) + ((lane >> 2) & 1)) * 576 + (tau >> 5) * 64 + (nn >> 3) * 32 + ((tau >> 4) & 1) * 16 + (nn & 7) + ((lane >> 3) & 1) * 8) * 2;
    asm volatile("" : "+v"(bp), "+v"(bl));
    float g0 = __uint_as_float(gg.x), g1 = __uint_as_float(gg.z), h0 = 1.f - g0, h1 = 1.f - g1;
    u32 ta[4];
#pragma unroll
    for (int j = 0; j < 4; ++j) ta[j] = tb + tr_lane_addr(lane, j);
    asm volatile("" : "+v"(oo), "+v"(oo2), "+v"(lo), "+v"(po), "+v"(po2), "+v"(ro0), "+v"(ro1), "+v"(ro2), "+v"(outo), "+v"(ta[0]), "+v"(ta[1]), "+v"(ta[2]), "+v"(ta[3]),
                 "+v"(g0), "+v"(g1), "+v"(h0), "+v"(h1));
    lds_wait_ge<0>(lbase + C_O, 4);
    u32 x1 = now() + M1_OWN;
    if ((int)(x1 - (t_last + M1)) < 0) x1 = t_last + M1;
    u32 tries = 0;
    {
      v4u u[8], B[16];
      v2u e;
#define LO(i, a, o) "global_load_dwordx4 %" #i ", %" #a ", %12 offset:" #o "*1024 sc1\n\t"
#define SWEEP_O(C, PRE, POST) asm volatile(PRE "global_load_dwordx2 %8, %11, %12 sc1\n\t" LO(0, 9, 0) LO(1, 9, 1) LO(2, 9, 2) LO(3, 9, 3) LO(4, 10, 0) LO(5, 10, 1) LO(6, 10, 2) LO(7, 10, 3) POST \
                     : C(u[0]), C(u[1]), C(u[2]), C(u[3]), C(u[4]), C(u[5]), C(u[6]), C(u[7]), C(e) \
                     : "v"(oo), "v"(oo2), "v"(lo), "s"(p.ws), "s"(need) : "memory")
      u64 need = ~0ull;
      wait_until<0>(x1);
      STAMP(5);
      SWEEP_O("=&v", "", "s_waitcnt vmcnt(21)");
#define BREAD(ks) _Pragma("unroll") for (int j = 0; j < 4; ++j) B[4 * j + (ks)] = trfrag(lds, ta[j], (ks) * 4096)
      BREAD(0);
      VMCNT(17); BREAD(1);
      VMCNT(13); BREAD(2);
      VMCNT(9);
      STAMP(9);
      BREAD(3);
      __builtin_amdgcn_sched_barrier(0);
      v4f acc[4];
#define PULL(a, v) (u32)__builtin_amdgcn_ds_bpermute((int)(a), (int)(v))
#define OPULL(ks) A[ks] = v4u{PULL(bp, u[2 * (ks)].x), PULL(bp, u[2 * (ks)].z), PULL(bp, u[2 * (ks) + 1].x), PULL(bp, u[2 * (ks) + 1].z)}
      v4u A[4];
#define OSTEP(ks) do { const v4u A_ = A[ks]; \
        _Pragma("unroll") for (int j = 0; j < 4; ++j) acc[j] = mfma(A_, B[4 * j + (ks)], (ks) == 0 ? v4f{0.f, 0.f, 0.f, 0.f} : acc[j]); } while (0)
#define MIX(j) pk_bf16(fmaf(w0, acc[j][0], w1 * acc[j][1]), fmaf(w2, acc[j][2], w3 * acc[j][3]))
#define WEIGHTS() do { const float me = __uint_as_float(el), wme = rcp(1.f + __builtin_amdgcn_exp2f(dpp<XOR1>(me) - me)); \
        w0 = dpp<0x00>(wme); w1 = dpp<0x55>(wme); w2 = dpp<0xAA>(wme); w3 = dpp<0xFF>(wme); } while (0)
      float w0, w1, w2, w3;
      asm volatile("s_waitcnt vmcnt(6)" : "+v"(e), "+v"(u[0]), "+v"(u[1]) :: "memory");
      u32 el = PULL(bl, e.x);
      OPULL(0);
      __builtin_amdgcn_sched_barrier(0);
      asm volatile("s_waitcnt vmcnt(4)" : "+v"(u[2]), "+v"(u[3]) :: "memory");
      OPULL(1);
      __builtin_amdgcn_sched_barrier(0);
      asm volatile("s_waitcnt vmcnt(2)" : "+v"(u[4]), "+v"(u[5]) :: "memory");
      OPULL(2);
      __builtin_amdgcn_sched_barrier(0);
      asm volatile("s_waitcnt vmcnt(0)" : "+v"(u[6]), "+v"(u[7]) :: "memory");
      OPULL(3);
      __builtin_amdgcn_sched_barrier(0);
      STAMP(6);
      OSTEP(0); OSTEP(1); OSTEP(2); OSTEP(3);
      WEIGHTS();
      u32 bad = e.y ^ T;
#pragma unroll
      for (int i = 0; i < 8; ++i) bad = tagbad(bad, u[i], T);
      need = ballot_nz(bad);
      if (__builtin_expect(need != 0, 0)) {
        NOUNROLL for (u32 spins = 0; spins < SPIN_CAP; ++spins) {
          tries += 10;
          SWEEP_O("+v", "s_sleep 2\n\ts_mov_b64 exec, %13\n\t", "s_mov_b64 exec, -1\n\ts_waitcnt vmcnt(0)");
          bad = e.y ^ T;
#pragma unroll
          for (int i = 0; i < 8; ++i) bad = tagbad(bad, u[i], T);
          need = ballot_nz(bad);
          if (need == 0) break;
        }
        el = PULL(bl, e.x);
        OPULL(0); OPULL(1); OPULL(2); OPULL(3);
        OSTEP(0); OSTEP(1); OSTEP(2); OSTEP(3);
        WEIGHTS();
      }
      st16_dev(p.ws, po, v4u{MIX(0), T, MIX(1), T});
      st16_dev(p.ws, po2, v4u{MIX(2), T, MIX(3), T});
    }
    STAMP(7);
    {
      v4u a[3];
      u64 nd[3];
#define LP(i, o, m) "s_mov_b64 exec, %" #m "\n\tglobal_load_dwordx4 %" #i ", %" #o ", %6 sc1\n\t"
      u32 x2 = now() + M2_OWN;
      if ((int)(x2 - (t_last + M2)) < 0) x2 = t_last + M2;
      wait_until<0>(x2);
      STAMP(10);
      asm volatile("global_load_dwordx4 %0, %3, %6 sc1\n\tglobal_load_dwordx4 %1, %4, %6 sc1\n\tglobal_load_dwordx4 %2, %5, %6 sc1\n\ts_waitcnt vmcnt(0)"
                   : "=&v"(a[0]), "=&v"(a[1]), "=&v"(a[2]) : "v"(ro0), "v"(ro1), "v"(ro2), "s"(p.ws) : "memory");
      if (__builtin_expect(ballot_nz(tagbad(tagbad(tagbad(0u, a[0], T), a[1], T), a[2], T)) != 0, 0)) {
        NOUNROLL for (u32 spins = 0; spins < SPIN_CAP; ++spins) {
#pragma unroll
          for (int i = 0; i < 3; ++i) nd[i] = ballot_nz(tagbad(0u, a[i], T));
          if ((nd[0] | nd[1] | nd[2]) == 0) break;
          tries += 1;
          asm volatile("s_sleep 2\n\t" LP(0, 3, 7) LP(1, 4, 8) LP(2, 5, 9) "s_mov_b64 exec, -1\n\ts_waitcnt vmcnt(0)\n.Lfd_end:"
                       : "+v"(a[0]), "+v"(a[1]), "+v"(a[2]) : "v"(ro0), "v"(ro1), "v"(ro2), "s"(p.ws), "s"(nd[0]), "s"(nd[1]), "s"(nd[2]) : "memory");
        }
      }
      const float v00 = g0 * (lo_f(a[0].x) + lo_f(a[1].x)) + h0 * lo_f(a[2].x), v01 = g1 * (hi_f(a[0].x) + hi_f(a[1].x)) + h1 * hi_f(a[2].x);
      const float v10 = g0 * (lo_f(a[0].z) + lo_f(a[1].z)) + h0 * lo_f(a[2].z), v11 = g1 * (hi_f(a[0].z) + hi_f(a[1].z)) + h1 * hi_f(a[2].z);
      const bool b2 = (lane & 4) != 0, b3 = (lane & 8) != 0;
      const float c0 = (b2 ? v01 : v00) + dpp<ROR4>(b2 ? v00 : v01), c1 = (b2 ? v11 : v10) + dpp<ROR4>(b2 ? v10 : v11);
      const float r = xg_sum((b3 ? c1 : c0) + dpp<ROR8>(b3 ? c0 : c1));
      if (lane < 16) *(unsigned short*)(p.out + outo) = (unsigned short)pk_bf16(r, r);
      if (tau == 0 && lane == 0) __hip_atomic_store((u32*)(p.ws + WS_EPOCH), T, __ATOMIC_RELAXED, __HIP_MEMORY_SCOPE_AGENT);
    }
    STAMP(8);
    (void)tries;
  }
  STAMP_FLUSH();
}

extern "C" void kernel_launch(void* const* d_in, const int* in_sizes, int n_in, void* d_out, int out_size, void* d_ws, size_t ws_size,
                              hipStream_t stream) {
  Params p;
  p.x = (const char*)d_in[0]; p.k = (const char*)d_in[1]; p.v = (const char*)d_in[2]; p.wq = (const char*)d_in[3]; p.wo = (const char*)d_in[4];
  p.wg = (const char*)d_in[6]; p.wu = (const char*)d_in[7]; p.wd = (const char*)d_in[8]; p.gw = (const char*)d_in[9];
  p.out = (char*)d_out; p.ws = (char*)d_ws;
  hipLaunchKernelGGL(fused_decode, dim3(256), dim3(512), 0, stream, p);
}
```
